```python
import torch

def _softmax(x, axis=-1):
    return torch.softmax(x, dim=axis)
def _gelu(x):
    return torch.nn.functional.gelu(x, approximate="tanh")
def _silu(x):
    return torch.nn.functional.silu(x)
def _sigmoid(x):
    return torch.sigmoid(x)
def _layernorm(x, axis=-1, eps=1e-5):
    m = x.mean(dim=axis, keepdim=True)
    v = x.var(dim=axis, keepdim=True, unbiased=False)
    return (x - m) / torch.sqrt(v + eps)
def _scatter_add(tgt, idx, src, axis=0):
    return tgt.clone().index_add_(axis, idx, src)
def _segment_sum(src, seg, n):
    out = torch.zeros((n,) + src.shape[1:], dtype=src.dtype)
    return out.index_add_(0, seg, src)
def _pad(x, pw):
    flat = tuple(v for pair in reversed(pw) for v in pair)
    return torch.nn.functional.pad(x, flat)

kernel_name = 'sequential_preLN__ssm_conv_s4d__mixer_token_mlp__prompt'

def setup_inputs():

    _w = (lambda a: (lambda t: t.to(torch.bfloat16) if t.is_floating_point() else t)(torch.as_tensor(a)))
    def _A_setup():
        Bsz, L, D, N = 1, 2048, 576, 128
        return {
            "u": _w((lambda *s: torch.randn(*s, dtype=torch.bfloat16) * 0.05)(Bsz, L, D)),
            "log_A_real": _w((lambda *s: torch.randn(*s, dtype=torch.bfloat16) * 6.4)(D, N) * 0.5),
            "A_imag": _w((lambda *s: torch.randn(*s, dtype=torch.bfloat16) * 0.05)(D, N) * 3.14),
            "C_real": _w((lambda *s: torch.randn(*s, dtype=torch.bfloat16) * 0.05)(D, N) / N ** 0.5),
            "C_imag": _w((lambda *s: torch.randn(*s, dtype=torch.bfloat16) * 0.05)(D, N) / N ** 0.5),
            "log_dt": _w((lambda *s: torch.randn(*s, dtype=torch.bfloat16) * 0.05)(D) * 0.5 - 4.0),
            "D_skip": _w((lambda *s: torch.randn(*s, dtype=torch.bfloat16) * 0.05)(D)),
        }
    _A_in = _A_setup()

    def _B_setup():
        B, S, D, F = 1, 2048, 576, 4608
        return {
            "x": _w((lambda *s: torch.randn(*s, dtype=torch.bfloat16) * 0.05)(B, S, D)),
            "w1": _w((lambda *s: torch.randn(*s, dtype=torch.bfloat16) * 0.4)(S, F) * (S ** -0.5)),
            "b1": _w((lambda *s: torch.randn(*s, dtype=torch.bfloat16) * 0.1)(F) * 0.02),
            "w2": _w((lambda *s: torch.randn(*s, dtype=torch.bfloat16) * 0.1)(F, S) * (F ** -0.5)),
            "b2": _w((lambda *s: torch.randn(*s, dtype=torch.bfloat16) * 0.1)(S) * 0.02),
        }
    _B_in = _B_setup()

    return {
        "A_u": _A_in["u"],
        "A_log_A_real": _A_in["log_A_real"],
        "A_A_imag": _A_in["A_imag"],
        "A_C_real": _A_in["C_real"],
        "A_C_imag": _A_in["C_imag"],
        "A_log_dt": _A_in["log_dt"],
        "A_D_skip": _A_in["D_skip"],
        "B_x": _B_in["x"],
        "B_w1": _B_in["w1"],
        "B_b1": _B_in["b1"],
        "B_w2": _B_in["w2"],
        "B_b2": _B_in["b2"],
    }

def reference(A_u, A_log_A_real, A_A_imag, A_C_real, A_C_imag, A_log_dt, A_D_skip, B_x, B_w1, B_b1, B_w2, B_b2):
    def _A_ref(u, log_A_real, A_imag, C_real, C_imag, log_dt, D_skip):
        L = u.shape[1]
        dt = torch.exp(log_dt)
        A = -torch.exp(log_A_real) + 1j * A_imag
        C = (C_real + 1j * C_imag) * dt[:, None]
        pos = (lambda x, dt: x.to(dt))(torch.arange(L), log_dt.dtype)
        P = torch.exp((A * dt[:, None])[:, :, None] * pos[None, None, :])
        K = 2.0 * torch.einsum("dn,dnl->dl", C, P).real
        L2 = 2 * L
        u_t = (lambda x, *d: x.permute(*d))(u, 0, 2, 1)
        Kf = (lambda x, *a, **k: torch.fft.rfft(x.float(), *a, **k))(K, L2)
        Uf = (lambda x, *a, **k: torch.fft.rfft(x.float(), *a, **k))(u_t, L2)
        y = (lambda x, *a, **k: torch.fft.irfft(x, *a, **k).to(torch.bfloat16))(Uf * Kf[None, :, :], L2)[:, :, :L]
        y = (lambda x, *d: x.permute(*d))(y, 0, 2, 1)
        return y + u * D_skip[None, None, :]

    def _B_ref(x, w1, b1, w2, b2):
        B, S, D = x.shape
        h = _layernorm(x, axis=-1)
        u = torch.einsum("bsd,sf->bdf", h, w1) + b1
        u = _gelu(u)
        y = torch.einsum("bdf,fs->bds", u, w2) + b2
        return (lambda x, *d: x.permute(*d))(y, 0, 2, 1)

    x = A_u
    h = x + _A_ref(_layernorm(x, axis=-1), A_log_A_real, A_A_imag, A_C_real, A_C_imag, A_log_dt, A_D_skip)
    h2 = h + _B_ref(_layernorm(h, axis=-1), B_w1, B_b1, B_w2, B_b2)
    return h2
```

```cpp
#include <hip/hip_runtime.h>
#include <hip/hip_bf16.h>
#include <stdint.h>

typedef unsigned short u16;
constexpr int L_ = 2048, D_ = 576, N_ = 128, F_ = 4608;

__device__ __forceinline__ float bf2f(u16 v) { return __uint_as_float(((unsigned)v) << 16); }
__device__ __forceinline__ u16 f2bf(float f) {
    unsigned u = __float_as_uint(f);
    u += 0x7FFFu + ((u >> 16) & 1u);
    return (u16)(u >> 16);
}
__device__ __forceinline__ float rbf(float f) { return bf2f(f2bf(f)); }

__device__ __forceinline__ float wave_sum(float v) {
    for (int o = 32; o > 0; o >>= 1) v += __shfl_xor(v, o);
    return v;
}

__device__ __forceinline__ void ln_row(float (&v)[9]) {
    float s = 0;
    for (int i = 0; i < 9; ++i) s += v[i];
    s = wave_sum(s);
    float mean = s * (1.0f / D_);
    float q = 0;
    for (int i = 0; i < 9; ++i) { float d = v[i] - mean; q += d * d; }
    q = wave_sum(q);
    float var = rbf(q * (1.0f / D_));
    float m = rbf(mean);
    float sd = rbf(sqrtf(rbf(var + 1e-5f)));
    for (int i = 0; i < 9; ++i) v[i] = rbf(rbf(v[i] - m) / sd);
}

__global__ void k_ln1(const u16* x, u16* u) {
    int row = blockIdx.x * 4 + (threadIdx.x >> 6), lane = threadIdx.x & 63;
    float v[9];
    for (int i = 0; i < 9; ++i) v[i] = bf2f(x[row * D_ + lane + 64 * i]);
    ln_row(v);
    for (int i = 0; i < 9; ++i) u[row * D_ + lane + 64 * i] = f2bf(v[i]);
}

__global__ void k_kgen(const u16* logA, const u16* Aim, const u16* Cre, const u16* Cim, const u16* logdt, float* K) {
    int d = blockIdx.x;
    __shared__ float zr[N_], zi[N_], cr[N_], ci[N_];
    float dt = rbf(expf(bf2f(logdt[d])));
    if (threadIdx.x < N_) {
        int n = threadIdx.x;
        float ar = -rbf(expf(bf2f(logA[d * N_ + n])));
        zr[n] = ar * dt; zi[n] = bf2f(Aim[d * N_ + n]) * dt;
        cr[n] = bf2f(Cre[d * N_ + n]) * dt; ci[n] = bf2f(Cim[d * N_ + n]) * dt;
    }
    __syncthreads();
    for (int l = threadIdx.x; l < L_; l += blockDim.x) {
        float p = rbf((float)l);
        float acc = 0;
        for (int n = 0; n < N_; ++n) {
            float e = expf(zr[n] * p);
            float s, c;
            sincosf(zi[n] * p, &s, &c);
            acc += e * (cr[n] * c - ci[n] * s);
        }
        K[d * L_ + l] = 2.0f * acc;
    }
}

__global__ void k_conv(const u16* x, const u16* u, const float* K, const u16* Dskip, u16* h) {
    int d = blockIdx.x;
    __shared__ float su[L_], sk[L_];
    for (int l = threadIdx.x; l < L_; l += blockDim.x) { su[l] = bf2f(u[l * D_ + d]); sk[l] = K[d * L_ + l]; }
    __syncthreads();
    float ds = bf2f(Dskip[d]);
    for (int l = threadIdx.x; l < L_; l += blockDim.x) {
        float acc = 0;
        for (int j = 0; j <= l; ++j) acc += sk[j] * su[l - j];
        float y = rbf(acc);
        float t = rbf(y + rbf(su[l] * ds));
        h[l * D_ + d] = f2bf(bf2f(x[l * D_ + d]) + t);
    }
}

__global__ void k_ln2(const u16* h, u16* hh) {
    int row = blockIdx.x * 4 + (threadIdx.x >> 6), lane = threadIdx.x & 63;
    float v[9];
    for (int i = 0; i < 9; ++i) v[i] = bf2f(h[row * D_ + lane + 64 * i]);
    ln_row(v);
    ln_row(v);
    for (int i = 0; i < 9; ++i) hh[row * D_ + lane + 64 * i] = f2bf(v[i]);
}

__global__ void k_gemm1(const u16* hh, const u16* w1, const u16* b1, u16* G) {
    int f = blockIdx.x * 256 + threadIdx.x;
    int d0 = blockIdx.y * 8;
    float acc[8] = {0};
    for (int s = 0; s < L_; ++s) {
        float w = bf2f(w1[(size_t)s * F_ + f]);
        for (int i = 0; i < 8; ++i) acc[i] += bf2f(hh[s * D_ + d0 + i]) * w;
    }
    float b = bf2f(b1[f]);
    for (int i = 0; i < 8; ++i) {
        float v = rbf(rbf(acc[i]) + b);
        float g = 0.5f * v * (1.0f + tanhf(0.7978845608028654f * (v + 0.044715f * v * v * v)));
        G[(size_t)(d0 + i) * F_ + f] = f2bf(g);
    }
}

__global__ void k_gemm2(const u16* G, const u16* w2, const u16* b2, const u16* h, u16* out) {
    int s = blockIdx.x * 256 + threadIdx.x;
    int d0 = blockIdx.y * 4;
    float acc[4] = {0};
    for (int f = 0; f < F_; ++f) {
        float w = bf2f(w2[(size_t)f * L_ + s]);
        for (int i = 0; i < 4; ++i) acc[i] += bf2f(G[(size_t)(d0 + i) * F_ + f]) * w;
    }
    float b = bf2f(b2[s]);
    for (int i = 0; i < 4; ++i) {
        float v = rbf(rbf(acc[i]) + b);
        out[s * D_ + d0 + i] = f2bf(bf2f(h[s * D_ + d0 + i]) + v);
    }
}

extern "C" void kernel_launch(void* const* d_in, const int* in_sizes, int n_in,
                              void* d_out, int out_size, void* d_ws, size_t ws_size,
                              hipStream_t stream) {
    const u16* x = (const u16*)d_in[0];
    const u16* logA = (const u16*)d_in[1];
    const u16* Aim = (const u16*)d_in[2];
    const u16* Cre = (const u16*)d_in[3];
    const u16* Cim = (const u16*)d_in[4];
    const u16* logdt = (const u16*)d_in[5];
    const u16* Dskip = (const u16*)d_in[6];
    const u16* w1 = (const u16*)d_in[8];
    const u16* b1 = (const u16*)d_in[9];
    const u16* w2 = (const u16*)d_in[10];
    const u16* b2 = (const u16*)d_in[11];
    char* ws = (char*)d_ws;
    u16* u = (u16*)ws;
    float* K = (float*)(ws + (4 << 20));
    u16* h = (u16*)(ws + (12 << 20));
    u16* hh = (u16*)(ws + (16 << 20));
    u16* G = (u16*)(ws + (20 << 20));
    k_ln1<<<L_ / 4, 256, 0, stream>>>(x, u);
    k_kgen<<<D_, 256, 0, stream>>>(logA, Aim, Cre, Cim, logdt, K);
    k_conv<<<D_, 256, 0, stream>>>(x, u, K, Dskip, h);
    k_ln2<<<L_ / 4, 256, 0, stream>>>(h, hh);
    k_gemm1<<<dim3(F_ / 256, D_ / 8), 256, 0, stream>>>(hh, w1, b1, G);
    k_gemm2<<<dim3(L_ / 256, D_ / 4), 256, 0, stream>>>(G, w2, b2, h, (u16*)d_out);
}
```

```cpp
#include <hip/hip_runtime.h>
#include <stdint.h>

typedef unsigned short u16;
typedef unsigned int u32;
typedef short s16x4 __attribute__((ext_vector_type(4)));
typedef short s16x8 __attribute__((ext_vector_type(8)));
typedef __bf16 bf16x8 __attribute__((ext_vector_type(8)));
typedef __bf16 bf16x2 __attribute__((ext_vector_type(2)));
typedef _Float16 f16x8 __attribute__((ext_vector_type(8)));
typedef _Float16 f16x2 __attribute__((ext_vector_type(2)));
typedef float f32x4 __attribute__((ext_vector_type(4)));
typedef float f32x2 __attribute__((ext_vector_type(2)));
typedef u32 u32x4 __attribute__((ext_vector_type(4)));
typedef u32 u32x2 __attribute__((ext_vector_type(2)));
#define LAS __attribute__((address_space(3)))
#define DEV __device__ __forceinline__

constexpr int L_ = 2048, D_ = 576, N_ = 128, F_ = 4608;

#define STAMP(kid, slot)
#define CSTAMP(kid, slot)
#define STAMP_ARG
#define STAMP_PASS

DEV float bf_lo(u32 w) { return __uint_as_float(w << 16); }
DEV float bf_hi(u32 w) { return __uint_as_float(w & 0xffff0000u); }
DEV float bf2f(u16 v) { return __uint_as_float(((u32)v) << 16); }
DEV u32 pk_bf16(float a, float b) { f32x2 v = {a, b}; return __builtin_bit_cast(u32, __builtin_convertvector(v, bf16x2)); }
DEV u32 pk_f16(float a, float b) { f32x2 v = {a, b}; return __builtin_bit_cast(u32, __builtin_convertvector(v, f16x2)); }
DEV float rbf(float a) { return bf_lo(pk_bf16(a, a)); }
DEV float h_lo(u32 w) { return (float)__builtin_bit_cast(f16x2, w)[0]; }
DEV float h_hi(u32 w) { return (float)__builtin_bit_cast(f16x2, w)[1]; }

template <int CTRL> DEV float dpp_add(float v) {
    return v + __builtin_bit_cast(float, __builtin_amdgcn_update_dpp(0, __builtin_bit_cast(int, v), CTRL, 0xf, 0xf, true));
}
DEV float wave_sum(float v) {
    v = dpp_add<0xB1>(v);
    v = dpp_add<0x4E>(v);
    v = dpp_add<0x141>(v);
    v = dpp_add<0x140>(v);
    {
        auto r = __builtin_amdgcn_permlane16_swap(__float_as_uint(v), __float_as_uint(v), false, false);
        v = __uint_as_float(r[0]) + __uint_as_float(r[1]);
    }
    {
        auto r = __builtin_amdgcn_permlane32_swap(__float_as_uint(v), __float_as_uint(v), false, false);
        v = __uint_as_float(r[0]) + __uint_as_float(r[1]);
    }
    return v;
}

DEV void block_sum8(float (&s)[8], float* red, int wave, int lane) {
#pragma unroll
    for (int r = 0; r < 8; ++r) s[r] = wave_sum(s[r]);
    if (lane == 0) {
        *(f32x4*)(red + wave * 8) = f32x4{s[0], s[1], s[2], s[3]};
        *(f32x4*)(red + wave * 8 + 4) = f32x4{s[4], s[5], s[6], s[7]};
    }
    __syncthreads();
    f32x4 a = *(const f32x4*)(red), b = *(const f32x4*)(red + 4);
#pragma unroll
    for (int w = 1; w < 5; ++w) { a += *(const f32x4*)(red + w * 8); b += *(const f32x4*)(red + w * 8 + 4); }
#pragma unroll
    for (int r = 0; r < 4; ++r) { s[r] = a[r]; s[r + 4] = b[r]; }
}

DEV void ln8(float (&a)[8][2], bool act, float* red, int wave, int lane) {
    float s[8];
#pragma unroll
    for (int r = 0; r < 8; ++r) s[r] = act ? a[r][0] + a[r][1] : 0.f;
    block_sum8(s, red, wave, lane);
    float mean[8], q[8];
#pragma unroll
    for (int r = 0; r < 8; ++r) {
        mean[r] = s[r] * (1.0f / D_);
        float d0 = a[r][0] - mean[r], d1 = a[r][1] - mean[r];
        q[r] = act ? d0 * d0 + d1 * d1 : 0.f;
    }
    block_sum8(q, red + 40, wave, lane);
#pragma unroll
    for (int r = 0; r < 8; ++r) {
        float var = rbf(q[r] * (1.0f / D_));
        float m = rbf(mean[r]);
        float sd = rbf(__builtin_amdgcn_sqrtf(rbf(var + 1e-5f)));
        float inv = __builtin_amdgcn_rcpf(sd);
        u32 c = pk_bf16(a[r][0] - m, a[r][1] - m);
        u32 o = pk_bf16(bf_lo(c) * inv, bf_hi(c) * inv);
        a[r][0] = bf_lo(o); a[r][1] = bf_hi(o);
    }
}

struct cf { float re, im; };
DEV cf cmul(cf a, cf b) { return cf{a.re * b.re - a.im * b.im, a.re * b.im + a.im * b.re}; }
DEV cf cexp_s(float zr2, float zi, float s) {
    float mag = __builtin_amdgcn_exp2f(zr2 * s);
    float th = zi * s;
    const float c_hi = 0.15915494309189535f, c_lo = 6.4206382e-9f;
    float hi = th * c_hi;
    float lo = __builtin_fmaf(th, c_hi, -hi) + th * c_lo;
    float rev = __builtin_amdgcn_fractf(hi) + lo;
    return cf{mag * __builtin_amdgcn_cosf(rev), mag * __builtin_amdgcn_sinf(rev)};
}

constexpr int TP = 528;
__global__ __launch_bounds__(320) void k_front1(const u16* __restrict__ x, const u16* __restrict__ logA, const u16* __restrict__ Aim,
                                                const u16* __restrict__ Cre, const u16* __restrict__ Cim, const u16* __restrict__ logdt,
                                                u16* __restrict__ u8, u16* __restrict__ KR, float* __restrict__ kscale STAMP_ARG) {
    __shared__ __attribute__((aligned(16))) char smem[63 * TP + 64];
    STAMP(0, 0);
    const int tid = threadIdx.x, lane = tid & 63, wave = tid >> 6;
    if (blockIdx.x < 256) {
        const int blk = blockIdx.x;
        const bool act = tid < 288;
        const int t = act ? tid : 0;
        float a[8][2];
#pragma unroll
        for (int r = 0; r < 8; ++r) { u32 w = *(const u32*)(x + (size_t)(blk * 8 + r) * D_ + 2 * t); a[r][0] = bf_lo(w); a[r][1] = bf_hi(w); }
        ln8(a, act, (float*)smem, wave, lane);
        if (act) {
            u32x4 o0 = {pk_f16(a[0][0], a[1][0]), pk_f16(a[2][0], a[3][0]), pk_f16(a[4][0], a[5][0]), pk_f16(a[6][0], a[7][0])};
            u32x4 o1 = {pk_f16(a[0][1], a[1][1]), pk_f16(a[2][1], a[3][1]), pk_f16(a[4][1], a[5][1]), pk_f16(a[6][1], a[7][1])};
            u32x4* dst = (u32x4*)(u8 + ((size_t)blk * D_ + 2 * t) * 8);
            dst[0] = o0; dst[1] = o1;
        }
    } else {
        const int d = blockIdx.x - 256;
        float* red = (float*)(smem + 63 * TP);
        float cr = 0, ci = 0, zr2 = 0, zi = 0, dt = 0;
        const int n = (wave & 1) * 64 + lane;
        if (wave < 4) {
            dt = rbf(__expf(bf2f(logdt[d])));
            float ar = rbf(__expf(bf2f(logA[d * N_ + n])));
            zr2 = -(ar * dt) * 1.4426950408889634f;
            zi = bf2f(Aim[d * N_ + n]) * dt;
            cr = bf2f(Cre[d * N_ + n]); ci = bf2f(Cim[d * N_ + n]);
            float m = fmaxf(fabsf(cr), fabsf(ci));
#pragma unroll
            for (int o = 32; o > 0; o >>= 1) m = fmaxf(m, __shfl_xor(m, o));
            if (lane == 0) red[wave] = m;
        }
        __syncthreads();
        float cs = fmaxf(red[0], red[1]);
        float inv = cs > 0.f ? 1.0f / cs : 0.f;
        if (tid == 0) kscale[d] = 2.0f * dt * cs;
        if (wave < 4) {
            cf C = {cr * inv, ci * inv};
            char* tb = smem + n * 4;
            auto put = [&](int row, cf v) { *(u32*)(tb + row * TP) = pk_f16(v.re, v.im); };
            if (wave < 2) {
                cf w = cexp_s(zr2, zi, 16.f), e1 = cexp_s(zr2, zi, 1.f);
                cf p = {1.f, 0.f}, q = C;
#pragma unroll
                for (int i = 0; i < 16; ++i) {
                    put(i, cf{p.re, -p.im}); put(16 + i, q);
                    if (i < 15) { p = cmul(p, w); q = cmul(q, e1); }
                }
            } else {
                cf e2 = cexp_s(zr2, zi, 2.f), e4 = cexp_s(zr2, zi, 4.f), e8 = cexp_s(zr2, zi, 8.f), b = cexp_s(zr2, zi, 256.f);
                cf q = cmul(C, b);
                { cf p = q;
#pragma unroll
                  for (int i = 0; i < 9; ++i) { put(32 + i, p); if (i < 8) p = cmul(p, e2); } }
#pragma unroll
                for (int a2 = 0; a2 < 2; ++a2) {
                    q = cmul(q, b);
                    cf p = q;
#pragma unroll
                    for (int i = 0; i < 5; ++i) { put(41 + a2 * 5 + i, p); if (i < 4) p = cmul(p, e4); }
                }
#pragma unroll
                for (int a2 = 0; a2 < 4; ++a2) {
                    q = cmul(q, b);
                    cf p = q;
#pragma unroll
                    for (int i = 0; i < 3; ++i) { put(51 + a2 * 3 + i, p); if (i < 2) p = cmul(p, e8); }
                }
            }
        }
        __syncthreads();
        if (wave < 4) {
            const int i = lane & 15, g = lane >> 4, qa = i >> 2, ra = i & 3;
            int arow;
            if (wave == 0) arow = 16 + i;
            else if (wave == 1) arow = 32 + min(2 * qa + ra, 8);
            else if (wave == 2) arow = 41 + (qa >> 1) * 5 + (qa & 1) * 2 + min(ra, 2);
            else arow = 51 + qa * 3 + min(ra, 2);
            f32x4 acc = {0, 0, 0, 0};
#pragma unroll
            for (int ks = 0; ks < 8; ++ks) {
                f16x8 A = *(const f16x8*)(smem + arow * TP + ks * 64 + g * 16);
                f16x8 B = *(const f16x8*)(smem + i * TP + ks * 64 + g * 16);
                acc = __builtin_amdgcn_mfma_f32_16x16x32_f16(A, B, acc, 0, 0, 0);
            }
            const int c = lane & 15, q = lane >> 4;
            u16* R = KR + (size_t)d * L_;
            if (wave == 0) {
                int lag0 = 16 * c + 4 * q;
                *(u32x2*)(R + 2044 - lag0) = u32x2{pk_f16(acc[3], acc[2]), pk_f16(acc[1], acc[0])};
            } else if (wave == 1) {
                int lag0 = 256 + 16 * c + 4 * q;
                *(u32x2*)(R + 2044 - lag0) = u32x2{pk_f16(acc[2], acc[1]), pk_f16(acc[0], acc[0])};
            } else if (wave == 2) {
                int lag0 = 512 + 256 * (q >> 1) + 8 * (q & 1) + 16 * c;
                *(u32x4*)(R + 2040 - lag0) = u32x4{pk_f16(acc[2], acc[2]), pk_f16(acc[1], acc[1]), pk_f16(acc[1], acc[0]), pk_f16(acc[0], acc[0])};
            } else {
                int lag0 = 1024 + 256 * q + 16 * c;
                u32 p00 = pk_f16(acc[0], acc[0]), p11 = pk_f16(acc[1], acc[1]), p22 = pk_f16(acc[2], acc[2]), p10 = pk_f16(acc[1], acc[0]);
                *(u32x4*)(R + 2032 - lag0) = u32x4{p22, p22, p11, p11};
                *(u32x4*)(R + 2040 - lag0) = u32x4{p11, p10, p00, p00};
            }
        }
    }
    STAMP(0, 1);
}

constexpr int KP = 4160;
constexpr int UP = 4640;
constexpr int CH_LDS = 4 * KP + 2 * UP;
__global__ __launch_bounds__(192) void k_conv(const u16* __restrict__ u8, const u16* __restrict__ KR, const float* __restrict__ kscale,
                                              u16* __restrict__ y8 STAMP_ARG) {
    extern __shared__ __attribute__((aligned(16))) char smem[];
    STAMP(1, 0);
    const int lane = threadIdx.x & 63, wave = threadIdx.x >> 6;
    const int grp = (blockIdx.x & 7) * 24 + (blockIdx.x >> 3);
    const int d = grp * 3 + wave;
    char* ks = smem + wave * CH_LDS;
    char* us = ks + 4 * KP;
    const u16* R = KR + (size_t)d * L_;
    u32x4 uv[4], kv[4]; u32x2 kh[4];
#pragma unroll
    for (int it = 0; it < 4; ++it) {
        int n = lane + 64 * it;
        uv[it] = *(const u32x4*)(u8 + ((size_t)n * D_ + d) * 8);
        kv[it] = *(const u32x4*)(R + 8 * n);
        kh[it] = *(const u32x2*)(R + min(8 * n + 8, L_ - 4));
    }
    const float scale = kscale[d];
    if (lane < 32) *(u32x4*)(us + lane * 16) = u32x4{0, 0, 0, 0};
    else *(u32x4*)(us + UP + (lane - 32) * 16) = u32x4{0, 0, 0, 0};
    if (lane == 0) *(u32x2*)(us + UP + 512) = u32x2{0, 0};
    if (lane < 16) *(u32x4*)(ks + (lane & 3) * KP + 4096 + (lane >> 2) * 16) = u32x4{0, 0, 0, 0};
#pragma unroll
    for (int it = 0; it < 4; ++it) {
        int n = lane + 64 * it;
        if (n == 255) kh[it] = u32x2{0, 0};
        u32 w0 = kv[it][0], w1 = kv[it][1], w2 = kv[it][2], w3 = kv[it][3], w4 = kh[it][0], w5 = kh[it][1];
        u32 s0 = __builtin_amdgcn_alignbit(w1, w0, 16), s1 = __builtin_amdgcn_alignbit(w2, w1, 16), s2 = __builtin_amdgcn_alignbit(w3, w2, 16),
            s3 = __builtin_amdgcn_alignbit(w4, w3, 16), s4 = __builtin_amdgcn_alignbit(w5, w4, 16);
        *(u32x4*)(ks + 3 * KP + n * 16) = u32x4{w0, w1, w2, w3};
        *(u32x4*)(ks + 2 * KP + n * 16) = u32x4{s0, s1, s2, s3};
        *(u32x4*)(ks + 1 * KP + n * 16) = u32x4{w1, w2, w3, w4};
        *(u32x4*)(ks + 0 * KP + n * 16) = u32x4{s1, s2, s3, s4};
        *(u32x4*)(us + 512 + n * 16) = uv[it];
        *(u32x2*)(us + UP + 512 + n * 16 + 8) = u32x2{uv[it][0], uv[it][1]};
        *(u32x2*)(us + UP + 512 + n * 16 + 16) = u32x2{uv[it][2], uv[it][3]};
    }
    __builtin_amdgcn_wave_barrier();
    asm volatile("s_waitcnt lgkmcnt(0)" ::: "memory");
    STAMP(1, 2); CSTAMP(1, 6);
    const int i = lane & 15, g = lane >> 4;
    const char* abase = ks + (i & 3) * KP + (2040 - 8 * (i >> 2) + 8 * g) * 2;
    const char* bbase = us + ((i & 1) ? 0 : UP) + 512 + (32 * (i >> 1) + 8 * g) * 2;
    f32x4 acc[8];
#pragma unroll
    for (int t = 0; t < 8; ++t) acc[t] = f32x4{0, 0, 0, 0};
#pragma unroll 2
    for (int jd = 0; jd < 8; ++jd) {
        f16x8 A[8], B[8];
#pragma unroll
        for (int t = 0; t < 8; ++t) {
            A[t] = *(const f16x8*)(abase - 64 * jd - 512 * t);
            B[t] = *(const f16x8*)(bbase - 64 * jd + 512 * t);
        }
#pragma unroll
        for (int id = 0; id < 8; ++id)
#pragma unroll
            for (int mm = 0; mm + id < 8; ++mm)
                acc[id + mm] = __builtin_amdgcn_mfma_f32_16x16x32_f16(A[id], B[mm], acc[id + mm], 0, 0, 0);
    }
    STAMP(1, 3); CSTAMP(1, 7);
#pragma unroll
    for (int t = 0; t < 8; ++t) {
        int unit = 32 * t + 4 * (i >> 1) + g;
        *(u32x2*)(y8 + ((size_t)unit * D_ + d) * 8 + (i & 1) * 4) =
            u32x2{pk_bf16(acc[t][0] * scale, acc[t][1] * scale), pk_bf16(acc[t][2] * scale, acc[t][3] * scale)};
    }
    STAMP(1, 1);
}

__global__ __launch_bounds__(320) void k_ln2(const u16* __restrict__ x, const u16* __restrict__ u8, const u16* __restrict__ y8,
                                             const u16* __restrict__ Dskip, u16* __restrict__ h, u16* __restrict__ hh8 STAMP_ARG) {
    __shared__ __attribute__((aligned(16))) float red[160];
    STAMP(2, 0);
    const int tid = threadIdx.x, lane = tid & 63, wave = tid >> 6;
    const int blk = blockIdx.x;
    const bool act = tid < 288;
    const int t = act ? tid : 0;
    u32 xw[8];
#pragma unroll
    for (int r = 0; r < 8; ++r) xw[r] = *(const u32*)(x + (size_t)(blk * 8 + r) * D_ + 2 * t);
    const u32x4* up = (const u32x4*)(u8 + ((size_t)blk * D_ + 2 * t) * 8);
    const u32x4* yp = (const u32x4*)(y8 + ((size_t)blk * D_ + 2 * t) * 8);
    u32x4 uu[2] = {up[0], up[1]}, yy[2] = {yp[0], yp[1]};
    u32 dsw = *(const u32*)(Dskip + 2 * t);
    float ds[2] = {bf_lo(dsw), bf_hi(dsw)};
    float a[8][2];
#pragma unroll
    for (int r = 0; r < 8; ++r) {
        float v[2];
#pragma unroll
        for (int c = 0; c < 2; ++c) {
            float uf = (r & 1) ? h_hi(uu[c][r >> 1]) : h_lo(uu[c][r >> 1]);
            float yf = (r & 1) ? bf_hi(yy[c][r >> 1]) : bf_lo(yy[c][r >> 1]);
            v[c] = rbf(yf + rbf(uf * ds[c]));
        }
        u32 hw = pk_bf16(bf_lo(xw[r]) + v[0], bf_hi(xw[r]) + v[1]);
        if (act) *(u32*)(h + (size_t)(blk * 8 + r) * D_ + 2 * t) = hw;
        a[r][0] = bf_lo(hw); a[r][1] = bf_hi(hw);
    }
    ln8(a, act, red, wave, lane);
    ln8(a, act, red + 80, wave, lane);
    if (act) {
        u32x4 o0 = {pk_bf16(a[0][0], a[1][0]), pk_bf16(a[2][0], a[3][0]), pk_bf16(a[4][0], a[5][0]), pk_bf16(a[6][0], a[7][0])};
        u32x4 o1 = {pk_bf16(a[0][1], a[1][1]), pk_bf16(a[2][1], a[3][1]), pk_bf16(a[4][1], a[5][1]), pk_bf16(a[6][1], a[7][1])};
        u32x4* dst = (u32x4*)(hh8 + ((size_t)blk * D_ + 2 * t) * 8);
        dst[0] = o0; dst[1] = o1;
    }
    STAMP(2, 1);
}

#define W_POL ""
#define A_POL ""
#define GLDS4(POL, lds, base, o0, o1, o2, o3) asm volatile("s_mov_b32 m0, %0\n\ts_nop 0\n\t" \
    "global_load_lds_dwordx4 %2, %1" POL "\n\tglobal_load_lds_dwordx4 %3, %1 offset:1024" POL "\n\t" \
    "global_load_lds_dwordx4 %4, %1 offset:2048" POL "\n\tglobal_load_lds_dwordx4 %5, %1 offset:3072" POL \
    :: "s"(lds), "s"(base), "v"(o0), "v"(o1), "v"(o2), "v"(o3) : "memory")
#define GLDS3(POL, lds, base, o0, o1, o2) asm volatile("s_mov_b32 m0, %0\n\ts_nop 0\n\t" \
    "global_load_lds_dwordx4 %2, %1" POL "\n\tglobal_load_lds_dwordx4 %3, %1 offset:1024" POL "\n\t" \
    "global_load_lds_dwordx4 %4, %1 offset:2048" POL \
    :: "s"(lds), "s"(base), "v"(o0), "v"(o1), "v"(o2) : "memory")
#define GLDS1(POL, lds, base, o0) asm volatile("s_mov_b32 m0, %0\n\ts_nop 0\n\t" \
    "global_load_lds_dwordx4 %2, %1" POL :: "s"(lds), "s"(base), "v"(o0) : "memory")
DEV u32 lds_addr(const void* p) { return (u32)(uintptr_t)(LAS const char*)p; }
DEV bf16x8 tr_frag(const char* p0, const char* p1) {
    s16x4 a = __builtin_amdgcn_ds_read_tr16_b64_v4i16((LAS s16x4*)p0);
    s16x4 b = __builtin_amdgcn_ds_read_tr16_b64_v4i16((LAS s16x4*)p1);
    return __builtin_bit_cast(bf16x8, __builtin_shufflevector(a, b, 0, 1, 2, 3, 4, 5, 6, 7));
}
#define WAIT_V(n) asm volatile("s_waitcnt vmcnt(" #n ")" ::: "memory")
#define SCHED() __builtin_amdgcn_sched_barrier(0)
#define WAIT_L0() asm volatile("s_waitcnt lgkmcnt(0)" ::: "memory")

DEV float gelu_tanh(float v) {
    float t = v * (1.0f + 0.044715f * v * v) * (-2.0f * 0.7978845608028654f * 1.4426950408889634f);
    return v * __builtin_amdgcn_rcpf(1.0f + __builtin_amdgcn_exp2f(t));
}

#define MFMA_BF(acc, a, b) asm volatile("v_mfma_f32_16x16x32_bf16 %0, %1, %2, %0" : "+v"(acc) : "v"(a), "v"(b))
#define DS_TR(dst, addr, off) asm volatile("ds_read_b64_tr_b16 %0, %1 offset:%2" : "=v"(dst) : "v"(addr), "i"(off))
#define DS_R128(dst, addr, off) asm volatile("ds_read_b128 %0, %1 offset:%2" : "=v"(dst) : "v"(addr), "i"(off))
#define BAR_L0() asm volatile("s_waitcnt lgkmcnt(0)\n\ts_barrier" ::: "memory")
struct Frag { u32x2 lo, hi; };
DEV u32x4 cat(const Frag& f) { return u32x4{f.lo[0], f.lo[1], f.hi[0], f.hi[1]}; }

constexpr int G1_LDS = 65536 + 49152;
struct G1Set { u32x4 W[4]; u32x4 H[3]; };
template <int SLOT, int KS, bool RD>
DEV void g1_half(f32x4 (&acc)[4][3], const G1Set& cur, G1Set& nxt, const u32 (&wa)[4][2], u32 ha) {
    constexpr int WO = SLOT * 16384 + KS * 8192, HO = SLOT * 12288 + KS * 6144;
    u32x2 w[4][2];
#pragma unroll
    for (int i = 0; i < 12; ++i) {
        MFMA_BF(acc[i / 3][i % 3], cur.W[i / 3], cur.H[i % 3]);
        if (RD) {
            if (i == 0) { DS_R128(nxt.H[0], ha, HO); DS_TR(w[0][0], wa[0][0], WO); }
            if (i == 1) { DS_TR(w[0][1], wa[0][1], WO); DS_R128(nxt.H[1], ha, HO + 256); }
            if (i == 2) { DS_R128(nxt.H[2], ha, HO + 512); }
            if (i == 3) { DS_TR(w[1][0], wa[1][0], WO); DS_TR(w[1][1], wa[1][1], WO); }
            if (i == 4) { DS_TR(w[2][0], wa[2][0], WO); DS_TR(w[2][1], wa[2][1], WO); }
            if (i == 5) { DS_TR(w[3][0], wa[3][0], WO); DS_TR(w[3][1], wa[3][1], WO); }
        }
    }
    if (RD) {
#pragma unroll
        for (int t = 0; t < 4; ++t) nxt.W[t] = u32x4{w[t][0][0], w[t][0][1], w[t][1][0], w[t][1][1]};
    }
}

__global__ __launch_bounds__(512) void k_gemm1(const u16* __restrict__ hh8, const u16* __restrict__ w1, const u16* __restrict__ b1,
                                               u16* __restrict__ G8 STAMP_ARG) {
    extern __shared__ __attribute__((aligned(16))) char smem[];
    STAMP(3, 0);
    const int lane = threadIdx.x & 63;
    const int wave8 = __builtin_amdgcn_readfirstlane(threadIdx.x >> 6);
    const int wave = wave8 & 3;
    const int id = (blockIdx.x & 7) * 27 + (blockIdx.x >> 3);
    const int f0 = (id / 6) * 128, d0 = (id % 6) * 96;
    constexpr int NS = L_ / 64;
    const u32 lbase = lds_addr(smem);
    if (wave8 >= 4) {
        u32 woff[4], hoff[3];
#pragma unroll
        for (int i = 0; i < 4; ++i) {
            int row = 16 * wave + 4 * i + (lane >> 4), c = lane & 15;
            int swz = ((row & 3) << 2) | ((row >> 2) & 3);
            woff[i] = row * (F_ * 2) + ((c ^ swz) << 4) + 4096 - 1024 * i;
        }
#pragma unroll
        for (int i = 0; i < 3; ++i) {
            int un = 192 * wave + 64 * i + lane;
            hoff[i] = (un / 96) * (D_ * 16) + (un % 96) * 16 + 4096 - 1024 * i;
        }
        const char* wsrc = (const char*)(w1 + f0) - 4096;
        const char* hsrc = (const char*)(hh8 + (size_t)d0 * 8) - 4096;
        auto issue = [&](int kt) {
            const int slot = kt & 3;
            const char* ws = wsrc + (size_t)kt * (64 * F_ * 2);
            const char* hs = hsrc + (size_t)kt * (8 * D_ * 16);
            GLDS4(W_POL, lbase + slot * 16384 + 4096 * wave, ws, woff[0], woff[1], woff[2], woff[3]);
            GLDS3(A_POL, lbase + 65536 + slot * 12288 + 3072 * wave, hs, hoff[0], hoff[1], hoff[2]);
        };
        issue(0); issue(1); issue(2); issue(3);
        WAIT_V(21); __builtin_amdgcn_s_barrier();
        for (int kt = 1; kt < NS - 3; ++kt) {
            WAIT_V(14);
            __builtin_amdgcn_s_barrier();
            issue(kt + 3);
        }
        WAIT_V(14); __builtin_amdgcn_s_barrier();
        WAIT_V(7); __builtin_amdgcn_s_barrier();
        WAIT_V(0); __builtin_amdgcn_s_barrier();
        return;
    }
    const int wf = wave >> 1, wd = wave & 1;
    const int g = lane >> 4, q = (lane & 15) >> 2, p = lane & 3, i16 = lane & 15;
    u32 wa[4][2];
#pragma unroll
    for (int t = 0; t < 4; ++t)
#pragma unroll
        for (int tau = 0; tau < 2; ++tau) {
            int row = 8 * g + 4 * tau + q;
            int swz = (q << 2) | ((2 * g + tau) & 3);
            int ch = 2 * (4 * wf + t) + (p >> 1);
            wa[t][tau] = lbase + row * 256 + ((ch ^ swz) << 4) + 8 * (p & 1);
        }
    const u32 ha = lbase + 65536 + g * 1536 + (48 * wd + i16) * 16;
    u32x2 bw[4];
#pragma unroll
    for (int t = 0; t < 4; ++t) bw[t] = *(const u32x2*)(b1 + f0 + 64 * wf + 16 * t + 4 * g);
    f32x4 acc[4][3];
#pragma unroll
    for (int t = 0; t < 4; ++t)
#pragma unroll
        for (int m = 0; m < 3; ++m) acc[t][m] = f32x4{0, 0, 0, 0};
    G1Set X, Y;
#pragma unroll
    for (int t = 0; t < 4; ++t) Y.W[t] = u32x4{0, 0, 0, 0};
#pragma unroll
    for (int m = 0; m < 3; ++m) Y.H[m] = u32x4{0, 0, 0, 0};
    asm volatile("s_nop 1");
    for (int kt = 0; kt < NS; kt += 4) {
        BAR_L0();
        if (kt == 0) { STAMP(3, 2); CSTAMP(3, 6); }
        g1_half<0, 0, true>(acc, Y, X, wa, ha); WAIT_L0();
        g1_half<0, 1, true>(acc, X, Y, wa, ha);
        BAR_L0();
        g1_half<1, 0, true>(acc, Y, X, wa, ha); WAIT_L0();
        g1_half<1, 1, true>(acc, X, Y, wa, ha);
        BAR_L0();
        g1_half<2, 0, true>(acc, Y, X, wa, ha); WAIT_L0();
        g1_half<2, 1, true>(acc, X, Y, wa, ha);
        BAR_L0();
        g1_half<3, 0, true>(acc, Y, X, wa, ha); WAIT_L0();
        g1_half<3, 1, true>(acc, X, Y, wa, ha);
    }
    WAIT_L0();
    g1_half<0, 0, false>(acc, Y, X, wa, ha);
    asm volatile("s_nop 7\n\ts_nop 3" ::: "memory");
#pragma unroll
    for (int t = 0; t < 4; ++t)
#pragma unroll
        for (int m = 0; m < 3; ++m) asm volatile("" : "+v"(acc[t][m]));
    STAMP(3, 3); CSTAMP(3, 7);
#pragma unroll
    for (int t = 0; t < 4; ++t) {
        int f = f0 + 64 * wf + 16 * t + 4 * g;
        float bb[4] = {bf_lo(bw[t][0]), bf_hi(bw[t][0]), bf_lo(bw[t][1]), bf_hi(bw[t][1])};
#pragma unroll
        for (int m = 0; m < 3; ++m) {
            float o[4];
#pragma unroll
            for (int r = 0; r < 4; ++r) o[r] = gelu_tanh(acc[t][m][r] + bb[r]);
            int d = d0 + 48 * wd + 16 * m + i16;
            *(u32x2*)(G8 + ((size_t)(f >> 3) * D_ + d) * 8 + (g & 1) * 4) = u32x2{pk_bf16(o[0], o[1]), pk_bf16(o[2], o[3])};
        }
    }
    STAMP(3, 1);
}

constexpr int G2_SLOT = 9216, G2_RING = 4;
struct G2Set { u32x4 W[4]; u32x4 A[5]; };
template <int SLOT, bool RD>
DEV void g2_step(f32x4 (&acc)[5][4], const G2Set& cur, G2Set& nxt, const u32 (&wa)[4], u32 ga) {
    constexpr int O = SLOT * G2_SLOT;
    u32x2 w[4][2];
#pragma unroll
    for (int i = 0; i < 20; ++i) {
        MFMA_BF(acc[i / 4][i % 4], cur.A[i / 4], cur.W[i % 4]);
        if (RD) {
            if (i == 0) DS_R128(nxt.A[0], ga, O);
            if (i >= 1 && i <= 8) DS_TR(w[(i - 1) >> 1][(i - 1) & 1], wa[(i - 1) >> 1], O + ((i - 1) & 1) * 512);
            if (i >= 9 && i <= 12) DS_R128(nxt.A[i - 8], ga, O + (i - 8) * 256);
        }
    }
    if (RD) {
#pragma unroll
        for (int t = 0; t < 4; ++t) nxt.W[t] = u32x4{w[t][0][0], w[t][0][1], w[t][1][0], w[t][1][1]};
    }
}
__global__ __launch_bounds__(512) void k_gemm2(const u16* __restrict__ G8, const u16* __restrict__ w2, const u16* __restrict__ b2,
                                               const u16* __restrict__ h, u16* __restrict__ out STAMP_ARG) {
    extern __shared__ __attribute__((aligned(16))) char smem[];
    STAMP(4, 0);
    const int lane = threadIdx.x & 63;
    const int wave8 = __builtin_amdgcn_readfirstlane(threadIdx.x >> 6);
    const int wave = wave8 & 3;
    const int r5 = blockIdx.x >> 3;
    const int s0 = ((blockIdx.x & 7) * 4 + (r5 >> 3)) * 64, d0 = (r5 & 7) * 72;
    constexpr int NS = F_ / 128;
    const u32 lbase = lds_addr(smem) + wave * (G2_SLOT * G2_RING);
    if (wave8 >= 4) {
        u32 woff[4], goff[5];
#pragma unroll
        for (int i = 0; i < 4; ++i) {
            int row = 8 * i + (lane >> 3), c = lane & 7;
            int swz = (row & 2) | ((row >> 1) & 4);
            woff[i] = row * (L_ * 2) + ((c ^ swz) << 4) + 4096 - 1024 * i;
        }
#pragma unroll
        for (int i = 0; i < 5; ++i) {
            int un = 64 * i + lane;
            goff[i] = (un / 80) * (D_ * 16) + min(un % 80, 71) * 16 + 4096 - 1024 * (i & 3);
        }
        const char* wsrc = (const char*)(w2 + s0) + (size_t)wave * (32 * L_ * 2) - 4096;
        const char* gsrc = (const char*)(G8 + (size_t)d0 * 8) + (size_t)wave * (4 * D_ * 16) - 4096;
        auto issue = [&](int kt) {
            u32 dst = lbase + (kt & 3) * G2_SLOT;
            const char* ws = wsrc + (size_t)kt * (128 * L_ * 2);
            const char* gs = gsrc + (size_t)kt * (16 * D_ * 16);
            GLDS4(W_POL, dst, ws, woff[0], woff[1], woff[2], woff[3]);
            GLDS4(A_POL, dst + 4096, gs, goff[0], goff[1], goff[2], goff[3]);
            GLDS1(A_POL, dst + 8192, gs, goff[4]);
        };
        issue(0); issue(1); issue(2); issue(3);
        WAIT_V(27); __builtin_amdgcn_s_barrier();
        for (int kt = 1; kt < NS - 3; ++kt) {
            WAIT_V(18);
            __builtin_amdgcn_s_barrier();
            issue(kt + 3);
        }
        WAIT_V(18); __builtin_amdgcn_s_barrier();
        WAIT_V(9); __builtin_amdgcn_s_barrier();
        WAIT_V(0); __builtin_amdgcn_s_barrier();
        return;
    }
    char* ring = smem + wave * (G2_SLOT * G2_RING);
    const int g = lane >> 4, q = (lane & 15) >> 2, p = lane & 3, i16 = lane & 15;
    u32 wa[4];
#pragma unroll
    for (int t = 0; t < 4; ++t) {
        int row = 8 * g + q;
        int swz = (row & 2) | ((row >> 1) & 4);
        int ch = 2 * t + (p >> 1);
        wa[t] = lbase + row * 128 + ((ch ^ swz) << 4) + 8 * (p & 1);
    }
    const u32 ga = lbase + 4096 + g * 1280 + i16 * 16;
    const int s = s0 + 16 * wave + i16;
    u32x2 hv[5];
#pragma unroll
    for (int m = 0; m < 5; ++m) hv[m] = *(const u32x2*)(h + (size_t)s * D_ + d0 + min(16 * m + 4 * g, 68));
    const float bb = bf2f(b2[s]);
    f32x4 acc[5][4];
#pragma unroll
    for (int m = 0; m < 5; ++m)
#pragma unroll
        for (int t = 0; t < 4; ++t) acc[m][t] = f32x4{0, 0, 0, 0};
    G2Set X, Y;
#pragma unroll
    for (int t = 0; t < 4; ++t) Y.W[t] = u32x4{0, 0, 0, 0};
#pragma unroll
    for (int m = 0; m < 5; ++m) Y.A[m] = u32x4{0, 0, 0, 0};
    asm volatile("s_nop 1");
    for (int kt = 0; kt < NS; kt += 4) {
        BAR_L0();
        if (kt == 0) { STAMP(4, 2); CSTAMP(4, 6); }
        g2_step<0, true>(acc, Y, X, wa, ga);
        BAR_L0();
        g2_step<1, true>(acc, X, Y, wa, ga);
        BAR_L0();
        g2_step<2, true>(acc, Y, X, wa, ga);
        BAR_L0();
        g2_step<3, true>(acc, X, Y, wa, ga);
    }
    WAIT_L0();
    g2_step<0, false>(acc, Y, X, wa, ga);
    asm volatile("s_nop 7\n\ts_nop 3" ::: "memory");
#pragma unroll
    for (int m = 0; m < 5; ++m)
#pragma unroll
        for (int t = 0; t < 4; ++t) asm volatile("" : "+v"(acc[m][t]));
    STAMP(4, 3); CSTAMP(4, 7);
    f32x4* redw = (f32x4*)ring;
#pragma unroll
    for (int t = 0; t < 4; ++t)
#pragma unroll
        for (int m = 0; m < 5; ++m) redw[(t * 5 + m) * 64 + lane] = acc[m][t];
    BAR_L0();
#pragma unroll
    for (int m = 0; m < 5; ++m) {
        f32x4 v = {0, 0, 0, 0};
#pragma unroll
        for (int w = 0; w < 4; ++w) v += ((const f32x4*)(smem + w * (G2_SLOT * G2_RING)))[(wave * 5 + m) * 64 + lane];
        float o[4];
        o[0] = bf_lo(hv[m][0]) + rbf(v[0] + bb); o[1] = bf_hi(hv[m][0]) + rbf(v[1] + bb);
        o[2] = bf_lo(hv[m][1]) + rbf(v[2] + bb); o[3] = bf_hi(hv[m][1]) + rbf(v[3] + bb);
        if (16 * m + 4 * g < 72) *(u32x2*)(out + (size_t)s * D_ + d0 + 16 * m + 4 * g) = u32x2{pk_bf16(o[0], o[1]), pk_bf16(o[2], o[3])};
    }
    STAMP(4, 1);
}

extern "C" void kernel_launch(void* const* d_in, const int* in_sizes, int n_in,
                              void* d_out, int out_size, void* d_ws, size_t ws_size,
                              hipStream_t stream) {
    const u16* x = (const u16*)d_in[0];
    const u16* logA = (const u16*)d_in[1];
    const u16* Aim = (const u16*)d_in[2];
    const u16* Cre = (const u16*)d_in[3];
    const u16* Cim = (const u16*)d_in[4];
    const u16* logdt = (const u16*)d_in[5];
    const u16* Dskip = (const u16*)d_in[6];
    const u16* w1 = (const u16*)d_in[8];
    const u16* b1 = (const u16*)d_in[9];
    const u16* w2 = (const u16*)d_in[10];
    const u16* b2 = (const u16*)d_in[11];
    char* ws = (char*)d_ws;
    constexpr size_t SZ = (size_t)L_ * D_ * 2;
    u16* u8 = (u16*)(ws);
    u16* KR = (u16*)(ws + SZ);
    u16* y8 = (u16*)(ws + 2 * SZ);
    u16* hh8 = (u16*)(ws + 3 * SZ);
    u16* h = (u16*)(ws + 4 * SZ);
    u16* G8 = (u16*)(ws + 5 * SZ);
    float* kscale = (float*)(ws + 5 * SZ + (size_t)D_ * F_ * 2);
    (void)hipFuncSetAttribute((const void*)k_conv, hipFuncAttributeMaxDynamicSharedMemorySize, 3 * CH_LDS);
    (void)hipFuncSetAttribute((const void*)k_gemm1, hipFuncAttributeMaxDynamicSharedMemorySize, G1_LDS);
    (void)hipFuncSetAttribute((const void*)k_gemm2, hipFuncAttributeMaxDynamicSharedMemorySize, G2_SLOT * G2_RING * 4);
    k_front1<<<256 + D_, 320, 0, stream>>>(x, logA, Aim, Cre, Cim, logdt, u8, KR, kscale STAMP_PASS);
    k_conv<<<192, 192, 3 * CH_LDS, stream>>>(u8, KR, kscale, y8 STAMP_PASS);
    k_ln2<<<256, 320, 0, stream>>>(x, u8, y8, Dskip, h, hh8 STAMP_PASS);
    k_gemm1<<<216, 512, G1_LDS, stream>>>(hh8, w1, b1, G8 STAMP_PASS);
    k_gemm2<<<256, 512, G2_SLOT * G2_RING * 4, stream>>>(G8, w2, b2, h, (u16*)d_out STAMP_PASS);
}
```

```cpp
#include <hip/hip_runtime.h>
#include <stdint.h>

typedef unsigned short u16;
typedef unsigned int u32;
typedef short s16x4 __attribute__((ext_vector_type(4)));
typedef short s16x8 __attribute__((ext_vector_type(8)));
typedef __bf16 bf16x8 __attribute__((ext_vector_type(8)));
typedef __bf16 bf16x2 __attribute__((ext_vector_type(2)));
typedef _Float16 f16x8 __attribute__((ext_vector_type(8)));
typedef _Float16 f16x2 __attribute__((ext_vector_type(2)));
typedef float f32x4 __attribute__((ext_vector_type(4)));
typedef float f32x2 __attribute__((ext_vector_type(2)));
typedef u32 u32x4 __attribute__((ext_vector_type(4)));
typedef u32 u32x2 __attribute__((ext_vector_type(2)));
#define LAS __attribute__((address_space(3)))
#define DEV __device__ __forceinline__

constexpr int L_ = 2048, D_ = 576, N_ = 128, F_ = 4608;

#define STAMP(kid, slot)
#define CSTAMP(kid, slot)
#define STAMP_ARG
#define STAMP_PASS

DEV float bf_lo(u32 w) { return __uint_as_float(w << 16); }
DEV float bf_hi(u32 w) { return __uint_as_float(w & 0xffff0000u); }
DEV float bf2f(u16 v) { return __uint_as_float(((u32)v) << 16); }
DEV u32 pk_bf16(float a, float b) { f32x2 v = {a, b}; return __builtin_bit_cast(u32, __builtin_convertvector(v, bf16x2)); }
DEV u32 pk_f16(float a, float b) { f32x2 v = {a, b}; return __builtin_bit_cast(u32, __builtin_convertvector(v, f16x2)); }
DEV float rbf(float a) { return bf_lo(pk_bf16(a, a)); }
DEV float h_lo(u32 w) { return (float)__builtin_bit_cast(f16x2, w)[0]; }
DEV float h_hi(u32 w) { return (float)__builtin_bit_cast(f16x2, w)[1]; }

template <int CTRL> DEV float dpp_add(float v) {
    return v + __builtin_bit_cast(float, __builtin_amdgcn_update_dpp(0, __builtin_bit_cast(int, v), CTRL, 0xf, 0xf, true));
}
DEV float wave_sum(float v) {
    v = dpp_add<0xB1>(v);
    v = dpp_add<0x4E>(v);
    v = dpp_add<0x141>(v);
    v = dpp_add<0x140>(v);
    {
        auto r = __builtin_amdgcn_permlane16_swap(__float_as_uint(v), __float_as_uint(v), false, false);
        v = __uint_as_float(r[0]) + __uint_as_float(r[1]);
    }
    {
        auto r = __builtin_amdgcn_permlane32_swap(__float_as_uint(v), __float_as_uint(v), false, false);
        v = __uint_as_float(r[0]) + __uint_as_float(r[1]);
    }
    return v;
}

DEV u32 lds_addr(const void* p) { return (u32)(uintptr_t)(LAS const char*)p; }
DEV float swap32_add(float v) {
    auto r = __builtin_amdgcn_permlane32_swap(__float_as_uint(v), __float_as_uint(v), false, false);
    return __uint_as_float(r[0]) + __uint_as_float(r[1]);
}
DEV float swap16_add(float v) {
    auto r = __builtin_amdgcn_permlane16_swap(__float_as_uint(v), __float_as_uint(v), false, false);
    return __uint_as_float(r[0]) + __uint_as_float(r[1]);
}
DEV float sum_j(float v) { return swap32_add(swap16_add(dpp_add<0x128>(v))); }
DEV u32x4 tr8x8(const char* wbuf, int lane) {
    const int q = (lane & 15) >> 2, p = lane & 3;
    const char* a = wbuf + ((lane >> 4) * 16 + (p >> 1) * 8 + q) * 16 + (p & 1) * 8;
    s16x4 lo = __builtin_amdgcn_ds_read_tr16_b64_v4i16((LAS s16x4*)a);
    s16x4 hi = __builtin_amdgcn_ds_read_tr16_b64_v4i16((LAS s16x4*)(a + 64));
    return __builtin_bit_cast(u32x4, __builtin_shufflevector(lo, hi, 0, 1, 2, 3, 4, 5, 6, 7));
}

template <int LPR, int NI>
DEV void l2_prefetch(const char* base, u32 pitch, int first, int t, int nthr) {
    u32 v[NI];
#pragma unroll
    for (int i = 0; i < NI; ++i) {
        u32 ln = first + t + i * nthr;
        v[i] = *(const u32*)(base + (size_t)(ln / LPR) * pitch + (ln % LPR) * 128);
    }
#pragma unroll
    for (int i = 0; i < NI; ++i) asm volatile("" :: "v"(v[i]));
}

struct cf { float re, im; };
DEV cf cmul(cf a, cf b) { return cf{a.re * b.re - a.im * b.im, a.re * b.im + a.im * b.re}; }
DEV cf cexp_s(float zr2, float zi, float s) {
    float mag = __builtin_amdgcn_exp2f(zr2 * s);
    float th = zi * s;
    const float c_hi = 0.15915494309189535f, c_lo = 6.4206382e-9f;
    float hi = th * c_hi;
    float lo = __builtin_fmaf(th, c_hi, -hi) + th * c_lo;
    float rev = __builtin_amdgcn_fractf(hi) + lo;
    return cf{mag * __builtin_amdgcn_cosf(rev), mag * __builtin_amdgcn_sinf(rev)};
}
template <int CTRL> DEV float dpp_max(float v) {
    return fmaxf(v, __builtin_bit_cast(float, __builtin_amdgcn_update_dpp(0, __builtin_bit_cast(int, v), CTRL, 0xf, 0xf, true)));
}
DEV float wave_max(float v) {
    v = dpp_max<0xB1>(v); v = dpp_max<0x4E>(v); v = dpp_max<0x141>(v); v = dpp_max<0x140>(v);
    { auto r = __builtin_amdgcn_permlane16_swap(__float_as_uint(v), __float_as_uint(v), false, false); v = fmaxf(__uint_as_float(r[0]), __uint_as_float(r[1])); }
    { auto r = __builtin_amdgcn_permlane32_swap(__float_as_uint(v), __float_as_uint(v), false, false); v = fmaxf(__uint_as_float(r[0]), __uint_as_float(r[1])); }
    return v;
}

#define MFMA_F16(acc, a, b) asm volatile("v_mfma_f32_16x16x32_f16 %0, %1, %2, %0" : "+v"(acc) : "v"(a), "v"(b))
#define DS_R128(dst, addr, off) asm volatile("ds_read_b128 %0, %1 offset:%2" : "=v"(dst) : "v"(addr), "i"(off))
#define WAIT_L0() asm volatile("s_waitcnt lgkmcnt(0)" ::: "memory")
#define PF_DELAY 500
#define PF_NI 9
#define PFA_NI 4
#define W_POL ""
#define A_POL ""
#define GLDS4(POL, lds, base, o0, o1, o2, o3) asm volatile("s_mov_b32 m0, %0\n\ts_nop 0\n\t" \
    "global_load_lds_dwordx4 %2, %1" POL "\n\tglobal_load_lds_dwordx4 %3, %1 offset:1024" POL "\n\t" \
    "global_load_lds_dwordx4 %4, %1 offset:2048" POL "\n\tglobal_load_lds_dwordx4 %5, %1 offset:3072" POL \
    :: "s"(lds), "s"(base), "v"(o0), "v"(o1), "v"(o2), "v"(o3) : "memory")
#define GLDS3(POL, lds, base, o0, o1, o2) asm volatile("s_mov_b32 m0, %0\n\ts_nop 0\n\t" \
    "global_load_lds_dwordx4 %2, %1" POL "\n\tglobal_load_lds_dwordx4 %3, %1 offset:1024" POL "\n\t" \
    "global_load_lds_dwordx4 %4, %1 offset:2048" POL \
    :: "s"(lds), "s"(base), "v"(o0), "v"(o1), "v"(o2) : "memory")
#define GLDS1(POL, lds, base, o0) asm volatile("s_mov_b32 m0, %0\n\ts_nop 0\n\t" \
    "global_load_lds_dwordx4 %2, %1" POL :: "s"(lds), "s"(base), "v"(o0) : "memory")
DEV bf16x8 tr_frag(const char* p0, const char* p1) {
    s16x4 a = __builtin_amdgcn_ds_read_tr16_b64_v4i16((LAS s16x4*)p0);
    s16x4 b = __builtin_amdgcn_ds_read_tr16_b64_v4i16((LAS s16x4*)p1);
    return __builtin_bit_cast(bf16x8, __builtin_shufflevector(a, b, 0, 1, 2, 3, 4, 5, 6, 7));
}
#define WAIT_V(n) asm volatile("s_waitcnt vmcnt(" #n ")" ::: "memory")

DEV float gelu_tanh(float v) {
    float t = v * (1.0f + 0.044715f * v * v) * (-2.0f * 0.7978845608028654f * 1.4426950408889634f);
    return v * __builtin_amdgcn_rcpf(1.0f + __builtin_amdgcn_exp2f(t));
}

#define MFMA_BF(acc, a, b) asm volatile("v_mfma_f32_16x16x32_bf16 %0, %1, %2, %0" : "+v"(acc) : "v"(a), "v"(b))
#define DS_TR(dst, addr, off) asm volatile("ds_read_b64_tr_b16 %0, %1 offset:%2" : "=v"(dst) : "v"(addr), "i"(off))
#define BAR_L0() asm volatile("s_waitcnt lgkmcnt(0)\n\ts_barrier" ::: "memory")
struct Frag { u32x2 lo, hi; };
DEV u32x4 cat(const Frag& f) { return u32x4{f.lo[0], f.lo[1], f.hi[0], f.hi[1]}; }

constexpr int G1_LDS = 65536 + 49152;
struct G1Set { u32x4 W[4]; u32x4 H[3]; };
template <int SLOT, int KS, bool RD>
DEV void g1_half(f32x4 (&acc)[4][3], const G1Set& cur, G1Set& nxt, const u32 (&wa)[4][2], u32 ha) {
    constexpr int WO = SLOT * 16384 + KS * 8192, HO = SLOT * 12288 + KS * 6144;
    u32x2 w[4][2];
#pragma unroll
    for (int i = 0; i < 12; ++i) {
        MFMA_BF(acc[i / 3][i % 3], cur.W[i / 3], cur.H[i % 3]);
        if (RD) {
            if (i == 0) { DS_R128(nxt.H[0], ha, HO); DS_TR(w[0][0], wa[0][0], WO); }
            if (i == 1) { DS_TR(w[0][1], wa[0][1], WO); DS_R128(nxt.H[1], ha, HO + 256); }
            if (i == 2) { DS_R128(nxt.H[2], ha, HO + 512); }
            if (i == 3) { DS_TR(w[1][0], wa[1][0], WO); DS_TR(w[1][1], wa[1][1], WO); }
            if (i == 4) { DS_TR(w[2][0], wa[2][0], WO); DS_TR(w[2][1], wa[2][1], WO); }
            if (i == 5) { DS_TR(w[3][0], wa[3][0], WO); DS_TR(w[3][1], wa[3][1], WO); }
        }
    }
    if (RD) {
#pragma unroll
        for (int t = 0; t < 4; ++t) nxt.W[t] = u32x4{w[t][0][0], w[t][0][1], w[t][1][0], w[t][1][1]};
    }
}

constexpr int G2_GS = 18432;
constexpr int G2_LDS = 65536 + 4 * G2_GS;
constexpr int SLAB = 9216;
constexpr int KB_LDS = G2_LDS + 512;
struct G2Set { u32x4 W[2]; u32x4 A[9]; };
template <int SLOT, int KS, bool RD>
DEV void g2_half(f32x4 (&acc)[9][2], const G2Set& cur, G2Set& nxt, const u32 (&wa)[2][2], u32 ga0, u32 ga2) {
    constexpr int WO = SLOT * 16384 + KS * 8192, GO = (SLOT & 1) * G2_GS + KS * 9216;
    const u32 ga = SLOT >= 2 ? ga2 : ga0;
    u32x2 w[2][2];
#pragma unroll
    for (int i = 0; i < 18; ++i) {
        MFMA_BF(acc[i / 2][i % 2], cur.A[i / 2], cur.W[i % 2]);
        if (RD) {
            if (i == 0) DS_R128(nxt.A[0], ga, GO);
            if (i == 1) { DS_TR(w[0][0], wa[0][0], WO); DS_TR(w[0][1], wa[0][1], WO); }
            if (i == 2) { DS_TR(w[1][0], wa[1][0], WO); DS_TR(w[1][1], wa[1][1], WO); }
            if (i >= 3 && i <= 10) DS_R128(nxt.A[i - 2], ga, GO + (i - 2) * 256);
        }
    }
    if (RD) {
#pragma unroll
        for (int t = 0; t < 2; ++t) nxt.W[t] = u32x4{w[t][0][0], w[t][0][1], w[t][1][0], w[t][1][1]};
    }
}
#define GAS __attribute__((address_space(1)))
#define RLX_AGENT __ATOMIC_RELAXED, __HIP_MEMORY_SCOPE_AGENT
constexpr int SC1 = 16;
constexpr unsigned long long SPIN_LIMIT = 2000000ull;
constexpr int CTL_E3 = 0, CTL_E4 = 8192, CTL_XF = 16384;

DEV float row_stats9(float (&a)[9], f32x2* red, int wave, int lane, float& mean_out) {
    float s = ((a[0] + a[1]) + (a[2] + a[3])) + ((a[4] + a[5]) + (a[6] + a[7])) + a[8];
    const float mw = sum_j(s) * (1.0f / 72);
    float q = 0;
#pragma unroll
    for (int i = 0; i < 9; ++i) { float d = a[i] - mw; q = __builtin_fmaf(d, d, q); }
    q = sum_j(q);
    if (lane < 8) red[wave * 8 + lane] = f32x2{mw, q};
    BAR_L0();
    const f32x2 pj = red[lane];
    const float mean = sum_j(pj[0]) * (1.0f / 8);
    const float dj = pj[0] - mean;
    const float m2 = sum_j(__builtin_fmaf(72.0f * dj, dj, pj[1]));
    mean_out = mean;
    return m2 * (1.0f / D_);
}

constexpr int TP = 528;
constexpr int TBL = 57 * TP;
constexpr int KA_K = 3 * TBL;
constexpr int KCH = 4160;
constexpr int KA_U = KA_K + 3 * KCH;
constexpr int KA_WB = KA_U + 288 * 48;
constexpr int KA_LDS = KA_WB + 8192 + 512;
constexpr int SCH = 2 * 3088 + 2 * 2048;
constexpr int KA_RED = 3 * SCH, KA_OUT = KA_RED + 24576;
static_assert(KA_OUT + 12288 <= KA_K && KA_K % 16 == 0 && KA_RED % 16 == 0, "LDS map");

DEV u32 pk_add_h(u32 a, u32 b) { return __builtin_bit_cast(u32, __builtin_bit_cast(f16x2, a) + __builtin_bit_cast(f16x2, b)); }
DEV u32x4 ld4(const char* p) { const u32* w = (const u32*)p; return u32x4{w[0], w[1], w[2], w[3]}; }
DEV void put4(char* p, int cp, float v0, float v1, float v2, float v3) {
    const u32 p32 = pk_f16(v3, v2), p10 = pk_f16(v1, v0), p21 = pk_f16(v2, v1);
    *(u32x2*)(p + cp - 4) = u32x2{p32, p10};
    *(u16*)(p - 6) = (u16)p32;
    *(u32*)(p - 4) = p21;
    *(u16*)(p) = (u16)(p10 >> 16);
}
#define FENCE8(a) asm volatile("s_nop 7\n\ts_nop 3" : "+v"(a[0]), "+v"(a[1]), "+v"(a[2]), "+v"(a[3]), "+v"(a[4]), "+v"(a[5]), "+v"(a[6]), "+v"(a[7]) :: "memory")
struct B8 { u32x4 f[8]; };
template <int MODE>
DEV void cv_dense(f32x4 (&acc)[8], const u32x4& A0, const u32x4& A1, const B8& cur, B8& nxt, u32 ba) {
    u32x2 h[8][2];
#pragma unroll
    for (int k = 0; k < 15; ++k) {
        if (k < 8) MFMA_F16(acc[k], A0, cur.f[k]); else MFMA_F16(acc[k - 7], A1, cur.f[k - 8]);
        if (MODE == 1 && k < 8) DS_R128(nxt.f[k], ba, 1536 * k);
        if (MODE == 2 && k < 8) { DS_TR(h[k][0], ba, 512 * (k >> 1) + 16 * (1 - (k & 1))); DS_TR(h[k][1], ba, 512 * (k >> 1) + 128 + 16 * (1 - (k & 1))); }
    }
    if (MODE == 2) {
#pragma unroll
        for (int k = 0; k < 8; ++k) nxt.f[k] = u32x4{h[k][0][0], h[k][0][1], h[k][1][0], h[k][1][1]};
    }
}

__global__ __launch_bounds__(512, 2) void k_front(const u16* __restrict__ x, const u16* __restrict__ logA, const u16* __restrict__ Aim,
                                                  const u16* __restrict__ Cre, const u16* __restrict__ Cim, const u16* __restrict__ logdt,
                                                  const u16* __restrict__ Dskip, const u16* __restrict__ w1,
                                                  u16* u8, u16* __restrict__ y8, u32* __restrict__ ctl, u32* ep STAMP_ARG) {
    extern __shared__ __attribute__((aligned(16))) char smem[];
    STAMP(0, 0);
    const int tid = threadIdx.x, lane = tid & 63;
    const int wave = __builtin_amdgcn_readfirstlane(tid >> 6);
    const int blk = blockIdx.x, xg = blk & 7, r5 = blk >> 3;
    const bool conv = r5 < 24;
    const int d0 = conv ? (xg * 24 + r5) * 3 : 0;
    GAS u32* epg = (GAS u32*)ep;
    const int rr = lane & 7, j8 = lane >> 3;
    const int ro = (blk * 8 + rr) * D_ + wave * 72;
    const u32x4 xv = __builtin_nontemporal_load((const u32x4*)(x + ro + j8 * 8));
    const u16 x9 = __builtin_nontemporal_load(x + ro + 64 + j8);
    const u32 tag = __hip_atomic_load(epg, RLX_AGENT) + 1u;
    u32 pla[3], pai[3], pcr[3], pci[3]; u16 pdt[3], pds[3];
#pragma unroll
    for (int c = 0; c < 3; ++c) {
        const int o = (d0 + c) * N_ + 2 * lane;
        pla[c] = __builtin_nontemporal_load((const u32*)(logA + o)); pai[c] = __builtin_nontemporal_load((const u32*)(Aim + o));
        pcr[c] = __builtin_nontemporal_load((const u32*)(Cre + o)); pci[c] = __builtin_nontemporal_load((const u32*)(Cim + o));
        pdt[c] = logdt[d0 + c]; pds[c] = Dskip[d0 + c];
    }
    for (int o = tid * 16; o < 3 * KCH + 1536; o += 8192) *(u32x4*)(smem + KA_K + o) = u32x4{0, 0, 0, 0};
    if (tid < 24) *(u32x4*)(ctl + blk * 96 + tid * 4) = u32x4{0, 0, 0, 0};
    {
        float a[9];
#pragma unroll
        for (int i = 0; i < 4; ++i) { a[2 * i] = bf_lo(xv[i]); a[2 * i + 1] = bf_hi(xv[i]); }
        a[8] = bf2f(x9);
        STAMP(0, 2);
        float mean;
        const float var = row_stats9(a, (f32x2*)(smem + KA_WB + 8192), wave, lane, mean);
        const float m = rbf(mean);
        const float inv = __builtin_amdgcn_rcpf(rbf(__builtin_amdgcn_sqrtf(rbf(rbf(var) + 1e-5f))));
        u32 o[5];
#pragma unroll
        for (int i = 0; i < 5; ++i) {
            const u32 c = pk_bf16(a[2 * i] - m, a[i < 4 ? 2 * i + 1 : 8] - m);
            const u32 n = pk_bf16(bf_lo(c) * inv, bf_hi(c) * inv);
            o[i] = pk_f16(bf_lo(n), bf_hi(n));
        }
        char* wb = smem + KA_WB + wave * 1024;
        *(u32x4*)(wb + lane * 16) = u32x4{o[0], o[1], o[2], o[3]};
        const int uo = (blk * D_ + wave * 72) * 8;
        const __amdgpu_buffer_rsrc_t rs_u = __builtin_amdgcn_make_buffer_rsrc(u8, 0, L_ * D_ * 2, 0x00020000);
        __builtin_amdgcn_raw_buffer_store_b16((u16)o[4], rs_u, (uo + 512 + lane) * 2, 0, SC1);
        __builtin_amdgcn_raw_buffer_store_b128(tr8x8(wb, lane), rs_u, (uo + lane * 8) * 2, 0, SC1);
    }
    const int hf = wave & 1, grp = wave >> 1;
    float zr2[3], zi[3], ksc[3], dsr[3]; cf C[3];
#pragma unroll
    for (int c = 0; c < 3; ++c) {
        const float dt = rbf(__expf(bf2f(pdt[c]))), dsk = bf2f(pds[c]);
        const float ar = rbf(__expf(hf ? bf_hi(pla[c]) : bf_lo(pla[c])));
        zr2[c] = -(ar * dt) * 1.4426950408889634f;
        zi[c] = (hf ? bf_hi(pai[c]) : bf_lo(pai[c])) * dt;
        const float cs = wave_max(fmaxf(fmaxf(fabsf(bf_lo(pcr[c])), fabsf(bf_hi(pcr[c]))), fmaxf(fabsf(bf_lo(pci[c])), fabsf(bf_hi(pci[c])))));
        ksc[c] = fmaxf(fmaxf(2.0f * dt * cs, fabsf(dsk) * (1.0f / 1024)), 1e-30f);
        const float rs = __builtin_amdgcn_rcpf(ksc[c]);
        dsr[c] = dsk * rs;
        const float inv = 2.0f * dt * rs;
        C[c] = cf{(hf ? bf_hi(pcr[c]) : bf_lo(pcr[c])) * inv, (hf ? bf_hi(pci[c]) : bf_lo(pci[c])) * inv};
    }
    WAIT_V(0);
    __builtin_amdgcn_s_barrier();
    if (wave == 0 && lane < 8) __hip_atomic_store(epg + 256 + lane * 256 + blk, tag, RLX_AGENT);
    STAMP(0, 3);
    if (!conv) {
        l2_prefetch<9, PFA_NI>((const char*)w1 + xg * 1152, F_ * 2, 0, (r5 - 24) * 512 + tid, 4096);
        if (PFA_NI == 4 && tid < 256) l2_prefetch<9, 1>((const char*)w1 + xg * 1152, F_ * 2, 16384, (r5 - 24) * 256 + tid, 0);
        STAMP(5, 0); STAMP(5, 1);
        return;
    }
    {
        char* tb = smem + (hf * 64 + lane) * 4;
        auto put = [&](int c, int row, cf v) { *(u32*)(tb + c * TBL + row * TP) = pk_f16(v.re, v.im); };
        if (grp < 2) {
            cf st[3], p[3];
#pragma unroll
            for (int c = 0; c < 3; ++c) {
                st[c] = cexp_s(zr2[c], grp ? zi[c] : -zi[c], grp ? 1.f : 16.f);
                p[c] = grp ? C[c] : cf{1.f, 0.f};
            }
            tb += grp * 16 * TP;
#pragma unroll
            for (int i = 0; i < 16; ++i)
#pragma unroll
                for (int c = 0; c < 3; ++c) { put(c, i, p[c]); if (i < 15) p[c] = cmul(p[c], st[c]); }
        } else if (grp == 2) {
#pragma unroll
            for (int c = 0; c < 3; ++c) {
                const cf e2 = cexp_s(zr2[c], zi[c], 2.f), e4 = cmul(e2, e2);
                cf p = cmul(C[c], cexp_s(zr2[c], zi[c], 256.f)), q = cmul(C[c], cexp_s(zr2[c], zi[c], 516.f));
#pragma unroll
                for (int i = 0; i < 9; ++i) { put(c, 32 + i, p); if (i < 8) p = cmul(p, e2); if (i < 4) { put(c, 41 + i, q); if (i < 3) q = cmul(q, e4); } }
            }
        } else {
#pragma unroll
            for (int c = 0; c < 3; ++c) {
                const cf e4 = cexp_s(zr2[c], zi[c], 4.f), e8 = cmul(e4, e4), e256 = cexp_s(zr2[c], zi[c], 256.f);
                cf q = cmul(C[c], cexp_s(zr2[c], zi[c], 772.f));
                cf p = cmul(cmul(q, e256), e4);
#pragma unroll
                for (int i = 0; i < 4; ++i) {
                    put(c, 45 + i, q); if (i < 3) q = cmul(q, e4);
                    put(c, 49 + 2 * i, p); put(c, 50 + 2 * i, cmul(p, e8)); if (i < 3) p = cmul(p, e256);
                }
            }
        }
    }
    BAR_L0();
    STAMP(0, 4);
    const int i16 = lane & 15, g = lane >> 4;
    const u32 lb = lds_addr(smem);
    if (wave < 6) {
        const int c = wave < 3 ? wave : wave - 3;
        const char* tb = smem + c * TBL + g * 16;
        char* kb = smem + KA_K + c * KCH;
        if (wave < 3) {
            const int r1 = 32 + min(2 * (i16 >> 2) + (i16 & 3), 8);
            f32x4 a0 = {0, 0, 0, 0}, a1 = {0, 0, 0, 0};
#pragma unroll
            for (int ks = 0; ks < 8; ++ks) {
                const f16x8 B = *(const f16x8*)(tb + i16 * TP + ks * 64);
                a0 = __builtin_amdgcn_mfma_f32_16x16x32_f16(*(const f16x8*)(tb + (16 + i16) * TP + ks * 64), B, a0, 0, 0, 0);
                a1 = __builtin_amdgcn_mfma_f32_16x16x32_f16(*(const f16x8*)(tb + r1 * TP + ks * 64), B, a1, 0, 0, 0);
            }
            if (lane == 0) a0[0] += c == 0 ? dsr[0] : c == 1 ? dsr[1] : dsr[2];
            char* pz = kb + (542 - 16 * i16 - 4 * g) * 2;
            put4(pz, 1120, a0[0], a0[1], a0[2], a0[3]);
            put4(pz - 512, 1120, a1[0], a1[0], a1[1], a1[2]);
            if (lane == 63) put4(kb + (542 - 512) * 2, 1120, a1[2], a1[2], a1[2], 0.f);
        } else {
            f32x4 a0 = {0, 0, 0, 0}, a1 = {0, 0, 0, 0};
#pragma unroll
            for (int ks = 0; ks < 8; ks += 2) {
                a0 = __builtin_amdgcn_mfma_f32_16x16x32_f16(*(const f16x8*)(tb + i16 * TP + ks * 64), *(const f16x8*)(tb + (41 + i16) * TP + ks * 64), a0, 0, 0, 0);
                a1 = __builtin_amdgcn_mfma_f32_16x16x32_f16(*(const f16x8*)(tb + i16 * TP + ks * 64 + 64), *(const f16x8*)(tb + (41 + i16) * TP + ks * 64 + 64), a1, 0, 0, 0);
            }
            a0 += a1;
            const int ph = i16 < 8 ? (i16 & 3) : 4 + (i16 & 1), a = i16 < 8 ? (i16 >> 2) : ((i16 - 8) >> 1);
            put4(kb + 2240 + ph * 320 + (62 - 16 * a - 4 * g) * 2, 160, a0[0], a0[1], a0[2], a0[3]);
        }
    } else if (wave == 7) {
        const __amdgpu_buffer_rsrc_t rs_f = __builtin_amdgcn_make_buffer_rsrc(ep + 256 + xg * 256, 0, 1024, 0x00020000);
        const unsigned long long t0 = __builtin_amdgcn_s_memrealtime();
        for (;;) {
            const u32x4 f = __builtin_amdgcn_raw_buffer_load_b128(rs_f, lane * 16, 0, SC1);
            if (__all(f[0] == tag && f[1] == tag && f[2] == tag && f[3] == tag)) break;
            if (__builtin_amdgcn_s_memrealtime() - t0 > SPIN_LIMIT) break;
            __builtin_amdgcn_s_sleep(2);
        }
        if (blk == 0 && lane == 0) __hip_atomic_store(epg, tag, RLX_AGENT);
        u32 go[3];
#pragma unroll
        for (int i = 0; i < 3; ++i) { const int e = 64 * i + lane; go[i] = (e / 3) * (D_ * 16) + (e % 3) * 16 + 4096 - 1024 * i; }
        const char* src = (const char*)(u8 + (size_t)d0 * 8) - 4096;
#pragma unroll
        for (int i = 0; i < 4; ++i) GLDS3(" sc1", lb + KA_U + 1536 + i * 3072, src + (size_t)i * (64 * D_ * 16), go[0], go[1], go[2]);
    }
    BAR_L0();
    STAMP(0, 5);
    f32x4* red = (f32x4*)(smem + KA_RED);
    char* ob = smem + KA_OUT;
    if (wave < 4) {
        f32x4 acc[8];
        const char* ka = smem + KA_K + (i16 & 1) * 1120 + (542 - 16 - (i16 & 14) + 8 * g) * 2;
        const u32 ub = lb + KA_U + (30 + 2 * i16 + g) * 48;
        B8 X, Y;
        if (wave < 3) {
#pragma unroll
            for (int t = 0; t < 8; ++t) { acc[t] = f32x4{0, 0, 0, 0}; asm volatile("" : "+v"(acc[t])); }
            u32x4 A[5][2], A16, A32, T2[2][3], T3[4];
            const char* kc = ka + wave * KCH;
#pragma unroll
            for (int jd = 0; jd < 5; ++jd) { A[jd][0] = ld4(kc - 64 * jd); A[jd][1] = ld4(kc - 64 * (jd + 8)); }
            A16 = ld4(kc - 64 * 16);
            const char* tc = smem + KA_K + wave * KCH + 2240 + (g >> 1) * 320 + (i16 & 1) * 160 + (62 - (i16 & 14) + 8 * (g & 1)) * 2;
#pragma unroll
            for (int b = 0; b < 3; ++b) { T2[0][b] = ld4(tc - 32 * b); T2[1][b] = ld4(tc + 640 - 32 * b); }
#pragma unroll
            for (int b = 0; b < 4; ++b) T3[b] = ld4(tc + 1280 - 32 * b);
            {
                const u32 k1024 = *(const u16*)(smem + KA_K + wave * KCH + 2240 + 3 * 320 + 31 * 2);
#pragma unroll
                for (int w = 0; w < 4; ++w) {
                    const int e = 8 * g + 2 * w - i16;
                    A32[w] = ((e == 12 || e == 13) ? k1024 : 0u) | ((e == 11 || e == 12) ? (k1024 << 16) : 0u);
                }
            }
            const u32 ba = ub + 16 * wave;
            const int q = i16 >> 2, p = lane & 3;
            const u32 sa2 = lb + wave * SCH + (g >> 1) * 3088 + (128 * (g & 1) + 16 * q + 4 * p) * 2;
            const u32 sa3 = lb + wave * SCH + 6176 + (g >> 1) * 2048 + (128 * (g & 1) + 16 * q + 4 * p) * 2;
#pragma unroll
            for (int jd = 0; jd < 5; ++jd) { asm volatile("" : "+v"(A[jd][0]), "+v"(A[jd][1])); }
            asm volatile("" : "+v"(A16), "+v"(A32), "+v"(T3[0]), "+v"(T3[1]), "+v"(T3[2]), "+v"(T3[3]));
            asm volatile("" : "+v"(T2[0][0]), "+v"(T2[0][1]), "+v"(T2[0][2]), "+v"(T2[1][0]), "+v"(T2[1][1]), "+v"(T2[1][2]));
            BAR_L0();
            STAMP(1, 0); STAMP(1, 2); CSTAMP(1, 6);
#pragma unroll
            for (int m = 0; m < 8; ++m) DS_R128(X.f[m], ba, 1536 * m);
            WAIT_L0();
            cv_dense<1>(acc, A[0][0], A[0][1], X, Y, ba - 192);
#pragma unroll
            for (int m = 0; m < 6; ++m) MFMA_F16(acc[m + 2], A16, X.f[m]);
#pragma unroll
            for (int m = 0; m < 4; ++m) MFMA_F16(acc[m + 4], A32, X.f[m]);
            WAIT_L0();
            cv_dense<1>(acc, A[1][0], A[1][1], Y, X, ba - 384); WAIT_L0();
            cv_dense<1>(acc, A[2][0], A[2][1], X, Y, ba - 576); WAIT_L0();
            cv_dense<1>(acc, A[3][0], A[3][1], Y, X, ba - 768);
            BAR_L0();
            cv_dense<2>(acc, A[4][0], A[4][1], X, Y, sa2); WAIT_L0();
            {
                u32x2 h[8][2];
                int cnt = 0;
#pragma unroll
                for (int b = 0; b < 3; ++b)
#pragma unroll
                    for (int k = 0; k < 8; ++k) {
                        const int pr = k >> 2, be = k & 3;
                        MFMA_F16(acc[2 + be + b], Y.f[2 * be + pr], T2[pr][b]);
                        if (cnt < 4) { DS_TR(h[cnt][0], sa2, 512 * (4 + (cnt >> 1)) + 16 * (1 - (cnt & 1))); DS_TR(h[cnt][1], sa2, 512 * (4 + (cnt >> 1)) + 128 + 16 * (1 - (cnt & 1))); }
                        else if (cnt < 8) { DS_TR(h[cnt][0], sa3, 512 * (cnt - 4)); DS_TR(h[cnt][1], sa3, 512 * (cnt - 4) + 128); }
                        ++cnt;
                    }
#pragma unroll
                for (int k = 0; k < 8; ++k) X.f[k] = u32x4{h[k][0][0], h[k][0][1], h[k][1][0], h[k][1][1]};
            }
            WAIT_L0();
            MFMA_F16(acc[6], X.f[0], T2[0][0]); MFMA_F16(acc[7], X.f[2], T2[0][0]);
            MFMA_F16(acc[4], X.f[4], T3[0]); MFMA_F16(acc[5], X.f[5], T3[0]);
            MFMA_F16(acc[6], X.f[1], T2[1][0]); MFMA_F16(acc[7], X.f[3], T2[1][0]);
            MFMA_F16(acc[5], X.f[4], T3[1]);
            MFMA_F16(acc[6], X.f[6], T3[0]); MFMA_F16(acc[7], X.f[0], T2[0][1]);
            MFMA_F16(acc[6], X.f[5], T3[1]); MFMA_F16(acc[7], X.f[1], T2[1][1]);
            MFMA_F16(acc[6], X.f[4], T3[2]); MFMA_F16(acc[7], X.f[7], T3[0]);
            MFMA_F16(acc[7], X.f[6], T3[1]); MFMA_F16(acc[7], X.f[5], T3[2]); MFMA_F16(acc[7], X.f[4], T3[3]);
            FENCE8(acc);
            STAMP(1, 3); CSTAMP(1, 7);
            BAR_L0();
            const float scale = wave == 0 ? ksc[0] : wave == 1 ? ksc[1] : ksc[2];
#pragma unroll
            for (int t = 0; t < 8; ++t) {
                const f32x4 v = (acc[t] + red[(wave * 8 + t) * 64 + lane]) * scale;
                *(u32x2*)(ob + (32 * t + 2 * i16 + (g >> 1)) * 48 + wave * 16 + (g & 1) * 8) = u32x2{pk_bf16(v[0], v[1]), pk_bf16(v[2], v[3])};
            }
        } else {
#pragma unroll
            for (int t = 0; t < 8; ++t) { acc[t] = f32x4{0, 0, 0, 0}; asm volatile("" : "+v"(acc[t])); }
            u32x4 A[3][3][2];
#pragma unroll
            for (int c = 0; c < 3; ++c)
#pragma unroll
                for (int k = 0; k < 3; ++k) {
                    A[c][k][0] = ld4(ka + c * KCH - 64 * (5 + k)); A[c][k][1] = ld4(ka + c * KCH - 64 * (13 + k));
                    asm volatile("" : "+v"(A[c][k][0]), "+v"(A[c][k][1]));
                }
            auto flush = [&](int c) {
                FENCE8(acc);
#pragma unroll
                for (int t = 0; t < 8; ++t) red[(c * 8 + t) * 64 + lane] = acc[t];
#pragma unroll
                for (int t = 0; t < 8; ++t) { acc[t] = f32x4{0, 0, 0, 0}; asm volatile("" : "+v"(acc[t])); }
                asm volatile("s_nop 1" ::: "memory");
            };
            const u32 ba = ub - 192 * 5;
            BAR_L0();
#pragma unroll
            for (int m = 0; m < 8; ++m) DS_R128(X.f[m], ba, 1536 * m);
            WAIT_L0();
            cv_dense<1>(acc, A[0][0][0], A[0][0][1], X, Y, ba - 192); WAIT_L0();
            cv_dense<1>(acc, A[0][1][0], A[0][1][1], Y, X, ba - 384); WAIT_L0();
            cv_dense<1>(acc, A[0][2][0], A[0][2][1], X, Y, ba + 16); WAIT_L0();
            flush(0);
            cv_dense<1>(acc, A[1][0][0], A[1][0][1], Y, X, ba + 16 - 192);
            BAR_L0();
            cv_dense<1>(acc, A[1][1][0], A[1][1][1], X, Y, ba + 16 - 384); WAIT_L0();
            cv_dense<1>(acc, A[1][2][0], A[1][2][1], Y, X, ba + 32); WAIT_L0();
            flush(1);
            cv_dense<1>(acc, A[2][0][0], A[2][0][1], X, Y, ba + 32 - 192); WAIT_L0();
            cv_dense<1>(acc, A[2][1][0], A[2][1][1], Y, X, ba + 32 - 384); WAIT_L0();
            cv_dense<0>(acc, A[2][2][0], A[2][2][1], X, Y, 0);
            flush(2);
            BAR_L0();
        }
    } else {
        if (wave == 7) WAIT_V(0);
        BAR_L0();
        if (wave < 7) {
            const int n = (wave - 4) * 64 + lane;
#pragma unroll
            for (int c = 0; c < 3; ++c) {
                const char* up = smem + KA_U + (29 + n) * 48 + 16 * c;
                u32 w[16], od[14], t2[14], v4[13];
#pragma unroll
                for (int k = 0; k < 4; ++k) { const u32x4 v = *(const u32x4*)(up + 48 * k); w[4 * k] = v[0]; w[4 * k + 1] = v[1]; w[4 * k + 2] = v[2]; w[4 * k + 3] = v[3]; }
#pragma unroll
                for (int k = 2; k < 14; ++k) od[k] = __builtin_amdgcn_alignbit(w[k + 1], w[k], 16);
#pragma unroll
                for (int k = 3; k < 14; ++k) t2[k] = pk_add_h(w[k], od[k - 1]);
#pragma unroll
                for (int k = 4; k < 13; ++k) v4[k] = pk_add_h(t2[k], t2[k - 1]);
                u32x4 s1, s2, so, se;
#pragma unroll
                for (int o = 0; o < 4; ++o) {
                    s1[o] = pk_add_h(od[10 + o], t2[10 + o]);
                    s2[o] = pk_add_h(v4[9 + o], w[7 + o]);
                    so[o] = pk_add_h(pk_add_h(v4[9 + o], t2[7 + o]), od[9 + o]);
                    se[o] = pk_add_h(pk_add_h(v4[6 + o], v4[4 + o]), w[2 + o]);
                }
                char* sp = smem + c * SCH;
                *(u32x4*)(sp + 16 + n * 16) = s1;
                *(u32x4*)(sp + 3088 + 16 + n * 16) = s2;
                if (wave < 6) { *(u32x4*)(sp + 6176 + n * 16) = so; *(u32x4*)(sp + 8224 + n * 16) = se; }
            }
        } else if (lane < 6) *(u32x4*)(smem + (lane >> 1) * SCH + (lane & 1) * 3088) = u32x4{0, 0, 0, 0};
        BAR_L0();
        BAR_L0();
    }
    BAR_L0();
#pragma unroll
    for (int it = 0; it < 2; ++it) {
        const int e = tid + 512 * it, n = e / 3, ch = e - 3 * n;
        if (e < 768) *(u32x4*)(y8 + ((size_t)n * D_ + d0 + ch) * 8) = *(const u32x4*)(ob + e * 16);
    }
    STAMP(1, 1); STAMP(0, 1);
}

__global__ __launch_bounds__(512, 2) void k_back(const u16* __restrict__ x, const u16* __restrict__ y8, const u16* __restrict__ w1,
                                                 const u16* __restrict__ b1, const u16* __restrict__ w2, const u16* __restrict__ b2,
                                                 u16* h, u16* hh8, u16* G8, u16* __restrict__ out, char* slabs, u32* ctl STAMP_ARG) {
    extern __shared__ __attribute__((aligned(16))) char smem[];
    STAMP(2, 0);
    const int lane = threadIdx.x & 63;
    const int wave8 = __builtin_amdgcn_readfirstlane(threadIdx.x >> 6);
    const int wave = wave8 & 3;
    const int xg = blockIdx.x & 7, r5 = blockIdx.x >> 3;
    const u32 lbase = lds_addr(smem);
    const int g = lane >> 4, q = (lane & 15) >> 2, p = lane & 3, i16 = lane & 15;
    GAS u32* ctlg = (GAS u32*)ctl;
    const __amdgpu_buffer_rsrc_t rs_h = __builtin_amdgcn_make_buffer_rsrc(h, 0, L_ * D_ * 2, 0x00020000);
    {
        const int blk = blockIdx.x, rr = lane & 7, j = lane >> 3;
        const int ro = (blk * 8 + rr) * D_ + wave8 * 72;
        const int uo = (blk * D_ + wave8 * 72) * 8;
        const u32x4 yu = *(const u32x4*)(y8 + uo + lane * 8);
        const u32x4 xv = *(const u32x4*)(x + ro + j * 8);
        const u16 y9 = y8[uo + 512 + lane], x9 = x[ro + 64 + j];
        char* wb = smem + 131072 + wave8 * 1024;
        *(u32x4*)(wb + lane * 16) = yu;
        const u32x4 yv = tr8x8(wb, lane);
        u32x4 hv;
        float a[9];
#pragma unroll
        for (int i = 0; i < 4; ++i) {
            hv[i] = pk_bf16(bf_lo(xv[i]) + bf_lo(yv[i]), bf_hi(xv[i]) + bf_hi(yv[i]));
            a[2 * i] = bf_lo(hv[i]); a[2 * i + 1] = bf_hi(hv[i]);
        }
        a[8] = rbf(bf2f(x9) + bf2f(y9));
        __builtin_amdgcn_raw_buffer_store_b128(hv, rs_h, (ro + j * 8) * 2, 0, SC1);
        __builtin_amdgcn_raw_buffer_store_b16((u16)(__float_as_uint(a[8]) >> 16), rs_h, (ro + 64 + j) * 2, 0, SC1);
        float mean;
        const float var = row_stats9(a, (f32x2*)(smem + 139264), wave8, lane, mean);
        const float r1 = __builtin_amdgcn_rsqf(var + 1e-5f);
        const float inv = r1 * __builtin_amdgcn_rsqf(var * r1 * r1 + 1e-5f);
#pragma unroll
        for (int i = 0; i < 9; ++i) a[i] = (a[i] - mean) * inv;
        *(u32x4*)(wb + lane * 16) = u32x4{pk_bf16(a[0], a[1]), pk_bf16(a[2], a[3]), pk_bf16(a[4], a[5]), pk_bf16(a[6], a[7])};
        const __amdgpu_buffer_rsrc_t rs_hh = __builtin_amdgcn_make_buffer_rsrc(hh8, 0, L_ * D_ * 2, 0x00020000);
        __builtin_amdgcn_raw_buffer_store_b16((u16)pk_bf16(a[8], a[8]), rs_hh, (uo + 512 + lane) * 2, 0, SC1);
        __builtin_amdgcn_raw_buffer_store_b128(tr8x8(wb, lane), rs_hh, (uo + lane * 8) * 2, 0, SC1);
        WAIT_V(0);
        __builtin_amdgcn_s_barrier();
        if (wave8 == 0 && lane < 32) __hip_atomic_fetch_add(ctlg + CTL_E3 + (lane * 8 + (blk >> 5)) * 32, 1u, RLX_AGENT);
    }
    STAMP(2, 1);
    const int kq = xg >> 1, tile = (xg & 1) * 32 + r5;
    const int s0 = (tile >> 2) * 128, d2 = (tile & 3) * 144;
    const bool g1 = r5 < 27;
    const int id = xg * 27 + r5;
    const int fg = id / 6, mt1 = id % 6;
    const int f0 = fg * 128, d0 = mt1 * 96;
    if (wave8 >= 4) {
        u32 woff[4];
        const char* w2src = (const char*)(w2 + s0) + (size_t)kq * (1152 * L_ * 2) - 4096;
        auto woffs = [&](int pitch) {
#pragma unroll
            for (int i = 0; i < 4; ++i) {
                int row = 16 * wave + 4 * i + (lane >> 4), c = lane & 15;
                int swz = ((row & 3) << 2) | ((row >> 2) & 3);
                woff[i] = row * pitch + ((c ^ swz) << 4) + 4096 - 1024 * i;
            }
        };
        auto issue_w = [&](const char* ws, int slot) { GLDS4(W_POL, lbase + slot * 16384 + 4096 * wave, ws, woff[0], woff[1], woff[2], woff[3]); };
        if (g1) {
            u32 hoff[3];
            woffs(F_ * 2);
#pragma unroll
            for (int i = 0; i < 3; ++i) {
                int un = 192 * wave + 64 * i + lane;
                hoff[i] = (un / 96) * (D_ * 16) + (un % 96) * 16 + 4096 - 1024 * i;
            }
            const char* wsrc = (const char*)(w1 + f0) - 4096;
            const char* hsrc = (const char*)(hh8 + (size_t)d0 * 8) - 4096;
            auto issue_h = [&](int kt) { GLDS3(" sc1", lbase + 65536 + (kt & 3) * 12288 + 3072 * wave, hsrc + (size_t)kt * (8 * D_ * 16), hoff[0], hoff[1], hoff[2]); };
            auto issue = [&](int kt) { issue_w(wsrc + (size_t)kt * (64 * F_ * 2), kt & 3); issue_h(kt); };
#pragma unroll
            for (int kt = 0; kt < 4; ++kt) issue_w(wsrc + (size_t)kt * (64 * F_ * 2), kt);
            __builtin_amdgcn_s_barrier();
            issue_h(0); issue_h(1); issue_h(2); issue_h(3);
            WAIT_V(9); __builtin_amdgcn_s_barrier();
            WAIT_V(6); __builtin_amdgcn_s_barrier(); issue(4);
            WAIT_V(10); __builtin_amdgcn_s_barrier(); issue(5);
            for (int kt = 3; kt < 29; ++kt) {
                WAIT_V(14);
                __builtin_amdgcn_s_barrier();
                issue(kt + 3);
            }
            woffs(L_ * 2);
            WAIT_V(14); __builtin_amdgcn_s_barrier(); issue_w(w2src, 0);
            WAIT_V(11); __builtin_amdgcn_s_barrier(); issue_w(w2src + 1 * (64 * L_ * 2), 1);
            WAIT_V(8); __builtin_amdgcn_s_barrier(); issue_w(w2src + 2 * (64 * L_ * 2), 2);
            __builtin_amdgcn_s_barrier(); issue_w(w2src + 3 * (64 * L_ * 2), 3);
        } else {
            woffs(L_ * 2);
            const char* pb = (const char*)w2 + (size_t)kq * (1152 * L_ * 2) + (xg & 1) * 2048;
            const int t = (r5 - 27) * 256 + (threadIdx.x - 256);
            l2_prefetch<16, 14>(pb, L_ * 2, 0, t, 1280);
            if (t < 512) l2_prefetch<16, 1>(pb, L_ * 2, 17920, t, 0);
#pragma unroll
            for (int kt = 0; kt < 4; ++kt) issue_w(w2src + (size_t)kt * (64 * L_ * 2), kt);
        }
        u32 goff[5];
#pragma unroll
        for (int i = 0; i < 5; ++i) {
            int un = min(64 * i + lane, 287);
            goff[i] = (2 * wave + un / 144) * (D_ * 16) + (un % 144) * 16 + 4096 - 1024 * (i & 3);
        }
        const char* gsrc = (const char*)(G8 + (size_t)d2 * 8) + (size_t)kq * (144 * D_ * 16) - 4096;
        auto issue_g = [&](int kt) {
            const char* gs = gsrc + (size_t)kt * (8 * D_ * 16);
            const u32 gd = lbase + 65536 + (kt & 3) * G2_GS + 4608 * wave;
            GLDS4(" sc1", gd, gs, goff[0], goff[1], goff[2], goff[3]);
            if (lane < 32) GLDS1(" sc1", gd + 4096, gs, goff[4]);
        };
        auto issue2 = [&](int kt) { issue_w(w2src + (size_t)kt * (64 * L_ * 2), kt & 3); issue_g(kt); };
        if (wave8 == 4) {
            const unsigned long long t0 = __builtin_amdgcn_s_memrealtime();
            GAS u32* c = ctlg + CTL_E4 + (((kq * 4 + (tile & 3)) * 4 + ((tile >> 2) & 3)) * 4 + (lane & 3)) * 32;
            for (;;) {
                int v = (int)__hip_atomic_load(c, RLX_AGENT);
                v += __builtin_amdgcn_update_dpp(0, v, 0xB1, 0xf, 0xf, true);
                v += __builtin_amdgcn_update_dpp(0, v, 0x4E, 0xf, 0xf, true);
                if (__builtin_amdgcn_readfirstlane(v) >= 54) break;
                if (__builtin_amdgcn_s_memrealtime() - t0 > SPIN_LIMIT) break;
                __builtin_amdgcn_s_sleep(2);
            }
        }
        asm volatile("" ::: "memory");
        __builtin_amdgcn_s_barrier();
        issue_g(0); issue_g(1); issue_g(2); issue_g(3);
        WAIT_V(15); __builtin_amdgcn_s_barrier();
        WAIT_V(10); __builtin_amdgcn_s_barrier(); issue2(4);
        WAIT_V(14); __builtin_amdgcn_s_barrier(); issue2(5);
        for (int kt = 3; kt < 15; ++kt) {
            WAIT_V(18);
            __builtin_amdgcn_s_barrier();
            issue2(kt + 3);
        }
        WAIT_V(18); __builtin_amdgcn_s_barrier();
        WAIT_V(9); __builtin_amdgcn_s_barrier();
        WAIT_V(0); __builtin_amdgcn_s_barrier();
    } else if (g1) {
        const int wf = wave >> 1, wd = wave & 1;
        u32 wa[4][2];
#pragma unroll
        for (int t = 0; t < 4; ++t)
#pragma unroll
            for (int tau = 0; tau < 2; ++tau) {
                int row = 8 * g + 4 * tau + q;
                int swz = (q << 2) | ((2 * g + tau) & 3);
                int ch = 2 * (4 * wf + t) + (p >> 1);
                wa[t][tau] = lbase + row * 256 + ((ch ^ swz) << 4) + 8 * (p & 1);
            }
        const u32 ha = lbase + 65536 + g * 1536 + (48 * wd + i16) * 16;
        u32x2 bw[4];
#pragma unroll
        for (int t = 0; t < 4; ++t) bw[t] = *(const u32x2*)(b1 + f0 + 64 * wf + 16 * t + 4 * g);
        f32x4 acc[4][3];
#pragma unroll
        for (int t = 0; t < 4; ++t)
#pragma unroll
            for (int m = 0; m < 3; ++m) { acc[t][m] = f32x4{0, 0, 0, 0}; asm volatile("" : "+v"(acc[t][m])); }
        G1Set X, Y;
#pragma unroll
        for (int t = 0; t < 4; ++t) { Y.W[t] = u32x4{0, 0, 0, 0}; asm volatile("" : "+v"(Y.W[t])); }
#pragma unroll
        for (int m = 0; m < 3; ++m) { Y.H[m] = u32x4{0, 0, 0, 0}; asm volatile("" : "+v"(Y.H[m])); }
        if (wave8 == 0) {
            const unsigned long long t0 = __builtin_amdgcn_s_memrealtime();
            GAS u32* c = ctlg + CTL_E3 + ((blockIdx.x & 31) * 8 + (lane & 7)) * 32;
            for (;;) {
                u32 v = __hip_atomic_load(c, RLX_AGENT);
                if (__all(v >= 32u)) break;
                if (__builtin_amdgcn_s_memrealtime() - t0 > SPIN_LIMIT) break;
                __builtin_amdgcn_s_sleep(1);
            }
        }
        STAMP(3, 0);
        asm volatile("" ::: "memory");
        __builtin_amdgcn_s_barrier();
        asm volatile("s_nop 1");
        for (int kt = 0; kt < 32; kt += 4) {
            BAR_L0();
            if (kt == 0) { STAMP(3, 2); CSTAMP(3, 6); }
            g1_half<0, 0, true>(acc, Y, X, wa, ha); WAIT_L0();
            g1_half<0, 1, true>(acc, X, Y, wa, ha);
            BAR_L0();
            g1_half<1, 0, true>(acc, Y, X, wa, ha); WAIT_L0();
            g1_half<1, 1, true>(acc, X, Y, wa, ha);
            BAR_L0();
            g1_half<2, 0, true>(acc, Y, X, wa, ha); WAIT_L0();
            g1_half<2, 1, true>(acc, X, Y, wa, ha);
            BAR_L0();
            g1_half<3, 0, true>(acc, Y, X, wa, ha); WAIT_L0();
            g1_half<3, 1, true>(acc, X, Y, wa, ha);
        }
        BAR_L0();
        g1_half<0, 0, false>(acc, Y, X, wa, ha);
        asm volatile("s_nop 7\n\ts_nop 3" ::: "memory");
#pragma unroll
        for (int t = 0; t < 4; ++t)
#pragma unroll
            for (int m = 0; m < 3; ++m) asm volatile("" : "+v"(acc[t][m]));
        STAMP(3, 3); CSTAMP(3, 7);
        const __amdgpu_buffer_rsrc_t rs_g = __builtin_amdgcn_make_buffer_rsrc(G8, 0, D_ * F_ * 2, 0x00020000);
        const int go = (((f0 + 64 * wf) / 8 + 2 * (g & 1) + (g >> 1)) * D_ + d0 + 48 * wd + i16) * 16;
#pragma unroll
        for (int t = 0; t < 4; t += 2) {
#pragma unroll
            for (int m = 0; m < 3; ++m) {
                u32 o[2][2];
#pragma unroll
                for (int k = 0; k < 2; ++k) {
                    const f32x4 v = acc[t + k][m];
                    o[k][0] = pk_bf16(gelu_tanh(v[0] + bf_lo(bw[t + k][0])), gelu_tanh(v[1] + bf_hi(bw[t + k][0])));
                    o[k][1] = pk_bf16(gelu_tanh(v[2] + bf_lo(bw[t + k][1])), gelu_tanh(v[3] + bf_hi(bw[t + k][1])));
                }
                auto r0 = __builtin_amdgcn_permlane16_swap(o[0][0], o[1][0], false, false);
                auto r1 = __builtin_amdgcn_permlane16_swap(o[0][1], o[1][1], false, false);
                __builtin_amdgcn_raw_buffer_store_b128(u32x4{r0[0], r1[0], r0[1], r1[1]}, rs_g, go + (2 * t * D_ + 16 * m) * 16, 0, SC1);
            }
        }
        STAMP(3, 4);
        WAIT_V(0);
        if (lane < 4) __hip_atomic_fetch_add(ctlg + CTL_E4 + ((((fg / 9) * 4 + (2 * mt1 + wd) / 3) * 4 + lane) * 4 + (fg & 3)) * 32, 1u, RLX_AGENT);
        STAMP(3, 1);
    }
    const __amdgpu_buffer_rsrc_t rs_s = __builtin_amdgcn_make_buffer_rsrc(slabs + (size_t)tile * (16 * SLAB), 0, 16 * SLAB, 0x00020000);
    GAS u32* fl = ctlg + CTL_XF + tile * 128;
    const int s = s0 + 32 * kq + i16;
    u32x2 hv[2][2];
    float bb[2];
    auto load_res = [&]() {
#pragma unroll
        for (int j = 0; j < 2; ++j)
#pragma unroll
            for (int t = 0; t < 2; ++t)
                hv[j][t] = __builtin_amdgcn_raw_buffer_load_b64(rs_h, ((s + 16 * t) * D_ + d2 + 16 * (j ? 8 : wave8) + 4 * g) * 2, 0, SC1);
        bb[0] = bf2f(b2[s]); bb[1] = bf2f(b2[s + 16]);
    };
    if (wave8 < 4) {
        u32 wa[2][2];
#pragma unroll
        for (int t = 0; t < 2; ++t)
#pragma unroll
            for (int tau = 0; tau < 2; ++tau) {
                int row = 8 * g + 4 * tau + q;
                int swz = (q << 2) | ((2 * g + tau) & 3);
                int ch = 2 * (2 * wave + t) + (p >> 1);
                wa[t][tau] = lbase + row * 256 + ((ch ^ swz) << 4) + 8 * (p & 1);
            }
        const u32 ga0 = lbase + 65536 + g * 2304 + i16 * 16, ga2 = ga0 + 2 * G2_GS;
        f32x4 acc[9][2];
#pragma unroll
        for (int m = 0; m < 9; ++m)
#pragma unroll
            for (int t = 0; t < 2; ++t) { acc[m][t] = f32x4{0, 0, 0, 0}; asm volatile("" : "+v"(acc[m][t])); }
        G2Set X, Y;
#pragma unroll
        for (int t = 0; t < 2; ++t) { Y.W[t] = u32x4{0, 0, 0, 0}; asm volatile("" : "+v"(Y.W[t])); }
#pragma unroll
        for (int m = 0; m < 9; ++m) { Y.A[m] = u32x4{0, 0, 0, 0}; asm volatile("" : "+v"(Y.A[m])); }
        asm volatile("" ::: "memory");
        __builtin_amdgcn_s_barrier();
        asm volatile("" ::: "memory");
        STAMP(4, 0);
        load_res();
        asm volatile("s_nop 1");
        for (int kt = 0; kt < 16; kt += 4) {
            BAR_L0();
            if (kt == 0) { STAMP(4, 2); CSTAMP(4, 6); }
            g2_half<0, 0, true>(acc, Y, X, wa, ga0, ga2); WAIT_L0();
            g2_half<0, 1, true>(acc, X, Y, wa, ga0, ga2);
            BAR_L0();
            g2_half<1, 0, true>(acc, Y, X, wa, ga0, ga2); WAIT_L0();
            g2_half<1, 1, true>(acc, X, Y, wa, ga0, ga2);
            BAR_L0();
            g2_half<2, 0, true>(acc, Y, X, wa, ga0, ga2); WAIT_L0();
            g2_half<2, 1, true>(acc, X, Y, wa, ga0, ga2);
            BAR_L0();
            g2_half<3, 0, true>(acc, Y, X, wa, ga0, ga2); WAIT_L0();
            g2_half<3, 1, true>(acc, X, Y, wa, ga0, ga2);
        }
        BAR_L0();
        g2_half<0, 0, true>(acc, Y, X, wa, ga0, ga2); WAIT_L0();
        g2_half<0, 1, true>(acc, X, Y, wa, ga0, ga2);
        BAR_L0();
        g2_half<1, 0, true>(acc, Y, X, wa, ga0, ga2); WAIT_L0();
        g2_half<1, 1, true>(acc, X, Y, wa, ga0, ga2);
        WAIT_L0();
        g2_half<0, 0, false>(acc, Y, X, wa, ga0, ga2);
        asm volatile("s_nop 7\n\ts_nop 3" ::: "memory");
#pragma unroll
        for (int m = 0; m < 9; ++m)
#pragma unroll
            for (int t = 0; t < 2; ++t) asm volatile("" : "+v"(acc[m][t]));
        STAMP(4, 3); CSTAMP(4, 7);
        if (wave == kq) {
            f32x4* own = (f32x4*)(smem + 32768);
#pragma unroll
            for (int i = 0; i < 18; ++i) own[i * 64 + lane] = acc[i >> 1][i & 1];
        } else {
            const int so = (wave * 4 + kq) * SLAB + lane * 16;
#pragma unroll
            for (int m = 0; m < 9; ++m)
                __builtin_amdgcn_raw_buffer_store_b128(u32x4{pk_bf16(acc[m][0][0], acc[m][0][1]), pk_bf16(acc[m][0][2], acc[m][0][3]),
                                                             pk_bf16(acc[m][1][0], acc[m][1][1]), pk_bf16(acc[m][1][2], acc[m][1][3])}, rs_s, so + m * 1024, 0, SC1);
            WAIT_V(0);
            if (lane == 0) __hip_atomic_store(fl + wave * 32 + kq, 1u, RLX_AGENT);
        }
        STAMP(4, 4);
    } else {
        load_res();
        if (wave8 == 4) {
            const unsigned long long t0 = __builtin_amdgcn_s_memrealtime();
            for (;;) {
                u32 f = __hip_atomic_load(fl + kq * 32 + (lane & 3), RLX_AGENT);
                if (__all(f == 1u || (lane & 3) == kq)) break;
                if (__builtin_amdgcn_s_memrealtime() - t0 > SPIN_LIMIT) break;
                __builtin_amdgcn_s_sleep(2);
            }
        }
    }
    BAR_L0();
    STAMP(4, 5);
    u32x4 part[2][3];
#pragma unroll
    for (int j = 0; j < 2; ++j) {
        const int po = j == 0 ? wave8 * 1024 + lane * 16 : (wave8 == 0 ? 8192 + lane * 16 : 0x40000000);
#pragma unroll
        for (int k = 0; k < 3; ++k) part[j][k] = __builtin_amdgcn_raw_buffer_load_b128(rs_s, (kq * 4 + ((kq + 1 + k) & 3)) * SLAB + po, 0, SC1);
    }
#pragma unroll
    for (int j = 0; j < 2; ++j) {
        if (j == 1 && wave8 != 0) break;
        const int m = j ? 8 : wave8;
#pragma unroll
        for (int t = 0; t < 2; ++t) {
            f32x4 v = ((const f32x4*)(smem + 32768))[(m * 2 + t) * 64 + lane];
#pragma unroll
            for (int k = 0; k < 3; ++k) v += f32x4{bf_lo(part[j][k][2 * t]), bf_hi(part[j][k][2 * t]), bf_lo(part[j][k][2 * t + 1]), bf_hi(part[j][k][2 * t + 1])};
            const u32 y0 = pk_bf16(v[0] + bb[t], v[1] + bb[t]), y1 = pk_bf16(v[2] + bb[t], v[3] + bb[t]);
            *(u32x2*)(out + (size_t)(s + 16 * t) * D_ + d2 + 16 * m + 4 * g) =
                u32x2{pk_bf16(bf_lo(hv[j][t][0]) + bf_lo(y0), bf_hi(hv[j][t][0]) + bf_hi(y0)), pk_bf16(bf_lo(hv[j][t][1]) + bf_lo(y1), bf_hi(hv[j][t][1]) + bf_hi(y1))};
        }
    }
    STAMP(4, 1);
}

extern "C" void kernel_launch(void* const* d_in, const int* in_sizes, int n_in,
                              void* d_out, int out_size, void* d_ws, size_t ws_size,
                              hipStream_t stream) {
    const u16* x = (const u16*)d_in[0];
    const u16* logA = (const u16*)d_in[1];
    const u16* Aim = (const u16*)d_in[2];
    const u16* Cre = (const u16*)d_in[3];
    const u16* Cim = (const u16*)d_in[4];
    const u16* logdt = (const u16*)d_in[5];
    const u16* Dskip = (const u16*)d_in[6];
    const u16* w1 = (const u16*)d_in[8];
    const u16* b1 = (const u16*)d_in[9];
    const u16* w2 = (const u16*)d_in[10];
    const u16* b2 = (const u16*)d_in[11];
    char* ws = (char*)d_ws;
    constexpr size_t SZ = (size_t)L_ * D_ * 2;
    u16* u8 = (u16*)(ws);
    u16* KR = (u16*)(ws + SZ);
    u16* y8 = (u16*)(ws + 2 * SZ);
    u16* hh8 = (u16*)(ws + 3 * SZ);
    u16* h = (u16*)(ws + 4 * SZ);
    u16* G8 = (u16*)(ws + 5 * SZ);
    float* kscale = (float*)(ws + 5 * SZ + (size_t)D_ * F_ * 2);
    u32* ctl = (u32*)(ws + (20u << 20));
    char* slabs = ws + (24u << 20);
    u32* ep = (u32*)(ws + (21u << 20));
    (void)hipFuncSetAttribute((const void*)k_front, hipFuncAttributeMaxDynamicSharedMemorySize, KA_LDS);
    (void)hipFuncSetAttribute((const void*)k_back, hipFuncAttributeMaxDynamicSharedMemorySize, KB_LDS);
    k_front<<<256, 512, KA_LDS, stream>>>(x, logA, Aim, Cre, Cim, logdt, Dskip, w1, u8, y8, ctl, ep STAMP_PASS);
    k_back<<<256, 512, KB_LDS, stream>>>(x, y8, w1, b1, w2, b2, h, hh8, G8, (u16*)d_out, slabs, ctl STAMP_PASS);
}
```

```cpp
#include <hip/hip_runtime.h>
#include <stdint.h>

typedef unsigned short u16;
typedef unsigned int u32;
typedef short s16x4 __attribute__((ext_vector_type(4)));
typedef short s16x8 __attribute__((ext_vector_type(8)));
typedef __bf16 bf16x8 __attribute__((ext_vector_type(8)));
typedef __bf16 bf16x2 __attribute__((ext_vector_type(2)));
typedef _Float16 f16x8 __attribute__((ext_vector_type(8)));
typedef _Float16 f16x2 __attribute__((ext_vector_type(2)));
typedef float f32x4 __attribute__((ext_vector_type(4)));
typedef float f32x2 __attribute__((ext_vector_type(2)));
typedef u32 u32x4 __attribute__((ext_vector_type(4)));
typedef u32 u32x2 __attribute__((ext_vector_type(2)));
#define LAS __attribute__((address_space(3)))
#define DEV __device__ __forceinline__

constexpr int L_ = 2048, D_ = 576, N_ = 128, F_ = 4608;

#define STAMP(id)
#define CSTAMP(id)
#define STAMP0(id)
#define CSTAMP0(id)
#define STAMP_INIT()
#define STAMP_DUMP()
#define STAMP_ARG
#define STAMP_PASS
#define LDS_REQ(n) (n)

DEV float bf_lo(u32 w) { return __uint_as_float(w << 16); }
DEV float bf_hi(u32 w) { return __uint_as_float(w & 0xffff0000u); }
DEV float bf2f(u16 v) { return __uint_as_float(((u32)v) << 16); }
DEV u32 pk_bf16(float a, float b) { f32x2 v = {a, b}; return __builtin_bit_cast(u32, __builtin_convertvector(v, bf16x2)); }
DEV u32 pk_f16(float a, float b) { f32x2 v = {a, b}; return __builtin_bit_cast(u32, __builtin_convertvector(v, f16x2)); }
DEV float rbf(float a) { return bf_lo(pk_bf16(a, a)); }
DEV float h_lo(u32 w) { return (float)__builtin_bit_cast(f16x2, w)[0]; }
DEV float h_hi(u32 w) { return (float)__builtin_bit_cast(f16x2, w)[1]; }

DEV u32 lds_addr(const void* p) { return (u32)(uintptr_t)(LAS const char*)p; }
template <int CTRL> DEV float dpp_get(float v) { return __builtin_bit_cast(float, __builtin_amdgcn_update_dpp(0, __builtin_bit_cast(int, v), CTRL, 0xf, 0xf, true)); }
DEV float swap32_add(float v) {
    auto r = __builtin_amdgcn_permlane32_swap(__float_as_uint(v), __float_as_uint(v), false, false);
    return __uint_as_float(r[0]) + __uint_as_float(r[1]);
}
DEV float swap16_add(float v) {
    auto r = __builtin_amdgcn_permlane16_swap(__float_as_uint(v), __float_as_uint(v), false, false);
    return __uint_as_float(r[0]) + __uint_as_float(r[1]);
}
DEV float sum_j(float v) { return swap32_add(swap16_add(v + dpp_get<0x128>(v))); }
DEV float sum_j4(float v) { return swap16_add(v + dpp_get<0x128>(v)); }
DEV u32 umax(u32 a, u32 b) { return a > b ? a : b; }
template <int CTRL> DEV u32 dpp_umax(u32 v) { return umax(v, (u32)__builtin_amdgcn_update_dpp(0, (int)v, CTRL, 0xf, 0xf, true)); }
DEV u32 wave_umax(u32 v) {
    v = dpp_umax<0xB1>(v); v = dpp_umax<0x4E>(v); v = dpp_umax<0x141>(v); v = dpp_umax<0x140>(v);
    { auto r = __builtin_amdgcn_permlane16_swap(v, v, false, false); v = umax(r[0], r[1]); }
    { auto r = __builtin_amdgcn_permlane32_swap(v, v, false, false); v = umax(r[0], r[1]); }
    return v;
}
DEV u32x4 tr8x8(const char* wbuf, int lane) {
    const int q = (lane & 15) >> 2, p = lane & 3;
    const char* a = wbuf + ((lane >> 4) * 16 + (p >> 1) * 8 + q) * 16 + (p & 1) * 8;
    s16x4 lo = __builtin_amdgcn_ds_read_tr16_b64_v4i16((LAS s16x4*)a);
    s16x4 hi = __builtin_amdgcn_ds_read_tr16_b64_v4i16((LAS s16x4*)(a + 64));
    return __builtin_bit_cast(u32x4, __builtin_shufflevector(lo, hi, 0, 1, 2, 3, 4, 5, 6, 7));
}

#define MFMA_BF(acc, a, b) asm volatile("v_mfma_f32_16x16x32_bf16 %0, %1, %2, %0" : "+v"(acc) : "v"(a), "v"(b))
#define MFMA_F16(acc, a, b) asm volatile("v_mfma_f32_16x16x32_f16 %0, %1, %2, %0" : "+v"(acc) : "v"(a), "v"(b))
#define DS_R128(dst, addr, off) asm volatile("ds_read_b128 %0, %1 offset:%2" : "=v"(dst) : "v"(addr), "i"(off))
#define DS_TR(dst, addr, off) asm volatile("ds_read_b64_tr_b16 %0, %1 offset:%2" : "=v"(dst) : "v"(addr), "i"(off))
#define WAIT_L0() asm volatile("s_waitcnt lgkmcnt(0)" ::: "memory")
#define WAIT_V(n) asm volatile("s_waitcnt vmcnt(" #n ")" ::: "memory")
#define BAR_L0() asm volatile("s_waitcnt lgkmcnt(0)\n\ts_barrier" ::: "memory")
#define BAR() asm volatile("s_barrier" ::: "memory")
#define PIN(x) asm volatile("" : "+v"(x))
#define FENCE6(a0, a1, a2, a3, a4, a5) asm volatile("s_nop 7\n\ts_nop 3" : "+v"(a0), "+v"(a1), "+v"(a2), "+v"(a3), "+v"(a4), "+v"(a5) :: "memory")
#define FENCE8(a) asm volatile("s_nop 7\n\ts_nop 3" : "+v"(a[0]), "+v"(a[1]), "+v"(a[2]), "+v"(a[3]), "+v"(a[4]), "+v"(a[5]), "+v"(a[6]), "+v"(a[7]) :: "memory")
#define GLDS4(POL, lds, base, o0, o1, o2, o3) asm volatile("s_mov_b32 m0, %0\n\ts_nop 0\n\t" \
    "global_load_lds_dwordx4 %2, %1" POL "\n\tglobal_load_lds_dwordx4 %3, %1 offset:1024" POL "\n\t" \
    "global_load_lds_dwordx4 %4, %1 offset:2048" POL "\n\tglobal_load_lds_dwordx4 %5, %1 offset:3072" POL \
    :: "s"(lds), "s"(base), "v"(o0), "v"(o1), "v"(o2), "v"(o3) : "memory")
#define GLDS3(POL, lds, base, o0, o1, o2) asm volatile("s_mov_b32 m0, %0\n\ts_nop 0\n\t" \
    "global_load_lds_dwordx4 %2, %1" POL "\n\tglobal_load_lds_dwordx4 %3, %1 offset:1024" POL "\n\t" \
    "global_load_lds_dwordx4 %4, %1 offset:2048" POL \
    :: "s"(lds), "s"(base), "v"(o0), "v"(o1), "v"(o2) : "memory")
#define GLDS1(POL, lds, base, o0) asm volatile("s_mov_b32 m0, %0\n\ts_nop 0\n\t" \
    "global_load_lds_dwordx4 %2, %1" POL :: "s"(lds), "s"(base), "v"(o0) : "memory")

#define GAS __attribute__((address_space(1)))
#define RLX_AGENT __ATOMIC_RELAXED, __HIP_MEMORY_SCOPE_AGENT
constexpr int SC1 = 16;
constexpr unsigned long long SPIN_LIMIT = 2000000ull;
constexpr int CTL_E3 = 0, CTL_E4 = 8192, CTL_XF = 16384;

constexpr size_t SZ = (size_t)L_ * D_ * 2;
constexpr size_t WS_CTL = 0, WS_EP = 98304, WS_U = 131072, WS_Y = WS_U + SZ, WS_HH = WS_Y + SZ, WS_H = WS_HH + SZ, WS_G = WS_H + SZ,
                 WS_SLAB = WS_G + (size_t)D_ * F_ * 2, WS_END = WS_SLAB + (size_t)64 * 16 * 9216;
static_assert(WS_END < (64u << 19), "the translation warm-up covers 64 x 512 KB");

template <int LPR, bool NT>
DEV void prefetch_lines(const char* base, u32 pitch, int first, int count, int t, int stride) {
    u32 prev = 0;
    for (int ln = t; ln < count; ln += stride) {
        const u32 l = first + ln;
        const u32* a = (const u32*)(base + (size_t)(l / LPR) * pitch + (l % LPR) * 128);
        const u32 v = NT ? __builtin_nontemporal_load(a) : *a;
        asm volatile("" :: "v"(prev));
        prev = v;
    }
    asm volatile("" :: "v"(prev));
}

template <int LPR>
DEV void prefetch_pair(u32 lds_junk, const char* base, u32 pitch, u32 l) {
    const u32 off = (l / LPR) * pitch + (l % LPR) * 128;
    asm volatile("s_mov_b32 m0, %0\n\ts_nop 0\n\tglobal_load_lds_dword %1, %2 nt\n\tglobal_load_lds_dword %1, %2" :: "s"(lds_junk), "v"(off), "s"(base) : "memory");
}
DEV void lds_arrive(volatile LAS u32* c, int lane) { WAIT_L0(); if (lane == 0) __hip_atomic_fetch_add((LAS u32*)c, 1u, __ATOMIC_RELAXED, __HIP_MEMORY_SCOPE_WORKGROUP); }
template <int SLEEP> DEV void lds_wait(volatile LAS u32* c, u32 n) { while (*c < n) { if (SLEEP) __builtin_amdgcn_s_sleep(SLEEP); } asm volatile("" ::: "memory"); }

template <class SYNC>
DEV float row_stats(const float (&a)[18], f32x2* red, int wave, int lane, float& mean_out, SYNC sync) {
    float s0 = a[0] + a[1], s1 = a[2] + a[3];
#pragma unroll
    for (int i = 4; i < 18; i += 2) { s0 += a[i]; s1 += a[i + 1]; }
    const float mw = sum_j(s0 + s1) * (1.0f / 144);
    float q0 = 0, q1 = 0;
#pragma unroll
    for (int i = 0; i < 18; i += 2) { const float d0 = a[i] - mw, d1 = a[i + 1] - mw; q0 = __builtin_fmaf(d0, d0, q0); q1 = __builtin_fmaf(d1, d1, q1); }
    const float q = sum_j(q0 + q1);
    if (lane < 8) red[wave * 8 + lane] = f32x2{mw, q};
    sync();
    const f32x2 pj = red[lane & 31];
    const float mean = sum_j4(pj[0]) * 0.25f;
    const float dj = pj[0] - mean;
    const float m2 = sum_j4(__builtin_fmaf(144.0f * dj, dj, pj[1]));
    mean_out = mean;
    return m2 * (1.0f / D_);
}

typedef f32x2 cf;
DEV cf cmul(cf p, cf q) {
    cf t, r;
    asm("v_pk_mul_f32 %0, %1, %2 op_sel:[0,0] op_sel_hi:[0,1]" : "=v"(t) : "v"(p), "v"(q));
    asm("v_pk_fma_f32 %0, %1, %2, %3 op_sel:[1,1,0] op_sel_hi:[1,0,1] neg_lo:[1,0,0]" : "=v"(r) : "v"(p), "v"(q), "v"(t));
    return r;
}
DEV u32 hmul(u32 p, u32 q) {
    u32 t, r;
    asm("v_pk_mul_f16 %0, %1, %2 op_sel:[0,0] op_sel_hi:[0,1]" : "=v"(t) : "v"(p), "v"(q));
    asm("v_pk_fma_f16 %0, %1, %2, %3 op_sel:[1,1,0] op_sel_hi:[1,0,1] neg_lo:[1,0,0]" : "=v"(r) : "v"(p), "v"(q), "v"(t));
    return r;
}
DEV u32 toh(cf v) { return pk_f16(v[0], v[1]); }
DEV cf cexp_s(float zr2, float zi, float s) {
    const float mag = __builtin_amdgcn_exp2f(zr2 * s);
    const float th = zi * s;
    const float c_hi = 0.15915494309189535f, c_lo = 6.4206382e-9f;
    const float hi = th * c_hi;
    const float lo = __builtin_fmaf(th, c_hi, -hi) + th * c_lo;
    const float rev = __builtin_amdgcn_fractf(hi) + lo;
    return cf{mag * __builtin_amdgcn_cosf(rev), mag * __builtin_amdgcn_sinf(rev)};
}
constexpr int TKS = 57 * 64;
constexpr int TBL = 8 * TKS;
constexpr int KA_K = 3 * TBL;
constexpr int KCH = 4160;
constexpr int KA_U = KA_K + 3 * KCH;
constexpr int KA_WB = KA_U + 288 * 48;
constexpr int KA_MISC = KA_WB + 8192;
constexpr int KA_JUNK = KA_MISC + 512;
constexpr int KA_LDS = KA_JUNK + 256;
constexpr int SCH = 2 * 3088 + 2 * 2048;
constexpr int KA_RED = 3 * SCH, KA_OUT = KA_RED + 24576;
static_assert(KA_OUT + 12288 <= KA_K && KA_K % 16 == 0 && KA_RED % 16 == 0, "LDS map");

DEV u32 pk_add_h(u32 a, u32 b) { return __builtin_bit_cast(u32, __builtin_bit_cast(f16x2, a) + __builtin_bit_cast(f16x2, b)); }
DEV u32x4 ld4(const char* p) { const u32* w = (const u32*)p; return u32x4{w[0], w[1], w[2], w[3]}; }
template <int CP>
DEV void put4(u32 p, float v0, float v1, float v2, float v3) {
    const u32 p32 = pk_f16(v3, v2), p10 = pk_f16(v1, v0), p21 = pk_f16(v2, v1);
    asm volatile("ds_write_b64 %0, %1 offset:%4\n\tds_write_b16 %0, %5 offset:2\n\tds_write_b32 %0, %2 offset:4\n\tds_write_b16_d16_hi %0, %3 offset:8"
                 :: "v"(p), "v"(u32x2{p32, p10}), "v"(p21), "v"(p10), "i"(CP + 4), "v"(p32) : "memory");
}
struct B8 { u32x4 f[8]; };
#define MFMA_F16_Z(acc, a, b) asm volatile("v_mfma_f32_16x16x32_f16 %0, %1, %2, 0" : "=&v"(acc) : "v"(a), "v"(b))
#define DS_W128(addr, src, off) asm volatile("ds_write_b128 %0, %1 offset:%2" :: "v"(addr), "v"(src), "i"(off) : "memory")
template <int MODE, bool ZERO = false, int FL = -1>
DEV void cv_dense(f32x4 (&acc)[8], const u32x4& A0, const u32x4& A1, const B8& cur, B8& nxt, u32 ba, const f32x4* old = nullptr, u32 ra = 0) {
    u32x2 h[8][2];
#pragma unroll
    for (int k = 0; k < 15; ++k) {
        if (k < 8) { if (ZERO) MFMA_F16_Z(acc[k], A0, cur.f[k]); else MFMA_F16(acc[k], A0, cur.f[k]); }
        else MFMA_F16(acc[k - 7], A1, cur.f[k - 8]);
        if (MODE == 1 && k < 8) DS_R128(nxt.f[k], ba, 1536 * k);
        if (MODE == 2 && k < 8) { DS_TR(h[k][0], ba, 512 * (k >> 1) + 16 * (1 - (k & 1))); DS_TR(h[k][1], ba, 512 * (k >> 1) + 128 + 16 * (1 - (k & 1))); }
        if (FL >= 0 && k >= 7) DS_W128(ra, old[k - 7], (FL * 8 + k - 7) * 1024);
    }
    if (MODE == 2) {
#pragma unroll
        for (int k = 0; k < 8; ++k) nxt.f[k] = u32x4{h[k][0][0], h[k][0][1], h[k][1][0], h[k][1][1]};
    }
}
struct KParam { u32 la, ai, cr, ci; u16 dt, ds; };
DEV KParam kparam_load(const u16* logA, const u16* Aim, const u16* Cre, const u16* Cim, const u16* logdt, const u16* Dskip, int d, int lane) {
    const int o = d * N_ + 2 * lane;
    return KParam{__builtin_nontemporal_load((const u32*)(logA + o)), __builtin_nontemporal_load((const u32*)(Aim + o)),
                  __builtin_nontemporal_load((const u32*)(Cre + o)), __builtin_nontemporal_load((const u32*)(Cim + o)),
                  __builtin_nontemporal_load(logdt + d), __builtin_nontemporal_load(Dskip + d)};
}
DEV void table_task(const KParam& k, int c, int hf, char* smem, f32x2* chp, int lane) {
    const int sh = hf ? 0 : 16, i16 = lane & 15;
    const float dt = rbf(__expf(bf2f(k.dt))), dsk = bf2f(k.ds);
    const float zr = -(rbf(__expf(bf_hi(k.la << sh))) * dt) * 1.4426950408889634f, zi = bf_hi(k.ai << sh) * dt;
    const float cs = __uint_as_float(wave_umax(umax(umax(k.cr & 0x7fff0000u, (k.cr << 16) & 0x7fff0000u), umax(k.ci & 0x7fff0000u, (k.ci << 16) & 0x7fff0000u))));
    const float ksc = fmaxf(fmaxf(2.0f * dt * cs, fabsf(dsk) * (1.0f / 1024)), 1e-30f);
    const float rs = __builtin_amdgcn_rcpf(ksc);
    const cf C = cf{bf_hi(k.cr << sh), bf_hi(k.ci << sh)} * (2.0f * dt * rs);
    char* tb = smem + c * TBL + (hf * 4 + (lane >> 4)) * TKS;
    auto put = [&](int row, u32 v) { *(u32*)(tb + row * 64 + ((i16 ^ (((-(row >> 2)) & 3) << 2)) << 2)) = v; };
    const cf e1 = cexp_s(zr, zi, 1.f), w1 = cexp_s(zr, -zi, 16.f), g1 = cexp_s(zr, zi, 256.f);
    const cf e2 = cmul(e1, e1), e3 = cmul(e2, e1), e4 = cmul(e2, e2);
    const cf w2 = cmul(w1, w1), w3 = cmul(w2, w1), w4 = cmul(w2, w2), w8 = cmul(w4, w4), w12 = cmul(w8, w4);
    const u32 f[4] = {0, toh(e1), toh(e2), toh(e3)};
    const u32 wf[4] = {0x00003c00u, toh(w1), toh(w2), toh(w3)}, wq[4] = {0, toh(w4), toh(w8), toh(w12)};
    u32 cq[5], gk[8];
    { cf t = C; cq[0] = toh(t);
#pragma unroll
      for (int a = 1; a < 5; ++a) { t = cmul(t, e4); cq[a] = toh(t); } }
    { cf t = g1; gk[1] = toh(t);
#pragma unroll
      for (int a = 2; a < 8; ++a) { t = cmul(t, g1); gk[a] = toh(t); } }
#pragma unroll
    for (int a = 0; a < 4; ++a)
#pragma unroll
        for (int b = 0; b < 4; ++b) {
            put(4 * a + b, a == 0 ? wf[b] : b == 0 ? wq[a] : hmul(wq[a], wf[b]));
            const u32 r0 = b == 0 ? cq[a] : hmul(cq[a], f[b]);
            put(16 + 4 * a + b, r0);
            if ((b & 1) == 0) put(48 + 2 * a + (b >> 1), hmul(r0, gk[1]));
        }
    put(56, hmul(cq[4], gk[1]));
#pragma unroll
    for (int i = 0; i < 4; ++i) {
        put(32 + i, hmul(cq[i + 1], gk[2])); put(36 + i, hmul(cq[i + 1], gk[3]));
        put(40 + 2 * i, hmul(cq[2], gk[4 + i])); put(41 + 2 * i, hmul(cq[4], gk[4 + i]));
    }
    if (lane == 0 && hf == 0) chp[c] = f32x2{ksc, dsk * rs};
}

#define POLL1_FIRST 26
#define POLL1_SLEEP 2
#define PFA 0
#define PFB 0
#define PFA_DELAY 330
__global__ __launch_bounds__(512, 2) void k_front(const u16* __restrict__ x, char* ws, const u16* __restrict__ logA, const u16* __restrict__ Aim,
                                                  const u16* __restrict__ Cre, const u16* __restrict__ Cim, const u16* __restrict__ logdt,
                                                  const u16* __restrict__ Dskip, const u16* __restrict__ w1 STAMP_ARG) {
    extern __shared__ __attribute__((aligned(16))) char smem[];
    constexpr int ST_FIRST = 0;
    asm volatile(".Lcode_begin:");
    const int tid = threadIdx.x, lane = tid & 63;
    const int wave8 = __builtin_amdgcn_readfirstlane(tid >> 6);
    const int wave = wave8;
    STAMP_INIT();
    asm volatile("" :: "s"(x), "s"(ws), "s"(logA), "s"(Aim), "s"(Cre), "s"(Cim), "s"(logdt), "s"(Dskip));
    const int blk = blockIdx.x, xg = blk & 7, r5 = blk >> 3;
    const bool conv = r5 < 27;
    const int d0 = conv ? (xg * 9 + r5 / 3) * 8 + min(3 * (r5 % 3), 5) : 0;
    u16* u8 = (u16*)(ws + WS_U);
    u16* y8 = (u16*)(ws + WS_Y);
    u32* ep = (u32*)(ws + WS_EP);
    GAS u32* epg = (GAS u32*)ep;
    const int rr = lane & 7, j8 = lane >> 3;
    u32x4 xv[2]; u16 x9[2]; u32 tag; KParam kp, kp2;
    if (wave < 4) {
        const int ro = (blk * 8 + rr) * D_ + wave * 144;
#pragma unroll
        for (int v = 0; v < 2; ++v) { xv[v] = __builtin_nontemporal_load((const u32x4*)(x + ro + 72 * v + j8 * 8)); x9[v] = __builtin_nontemporal_load(x + ro + 72 * v + 64 + j8); }
        tag = __hip_atomic_load(epg, RLX_AGENT) + 1u;
    } else {
        kp = kparam_load(logA, Aim, Cre, Cim, logdt, Dskip, d0 + ((wave >> 1) & 1), lane);
        kp2 = kparam_load(logA, Aim, Cre, Cim, logdt, Dskip, d0 + 2, lane);
    }
    volatile LAS u32* cnt = (volatile LAS u32*)(smem + KA_MISC + 256);
    if (tid < 6) cnt[tid] = 0;
    BAR_L0();
    f32x2* chp = (f32x2*)(smem + KA_MISC + 288);
    const int i16 = lane & 15, g = lane >> 4;
    const u32 lb = lds_addr(smem);
    const char* w1win = (const char*)w1 + ((27 * xg) / 6) * 256;
    if (wave < 4) {
        __builtin_amdgcn_s_setprio(2);
        float a[18];
#pragma unroll
        for (int v = 0; v < 2; ++v) {
#pragma unroll
            for (int i = 0; i < 4; ++i) { a[9 * v + 2 * i] = bf_lo(xv[v][i]); a[9 * v + 2 * i + 1] = bf_hi(xv[v][i]); }
            a[9 * v + 8] = bf2f(x9[v]);
        }
        PIN(a[0]); STAMP0(1);
        float mean;
        const float var = row_stats(a, (f32x2*)(smem + KA_MISC), wave, lane, mean, [&]() { lds_arrive(cnt, lane); lds_wait<0>(cnt, 4u); });
        const float m = rbf(mean);
        const float inv = __builtin_amdgcn_rcpf(rbf(__builtin_amdgcn_sqrtf(rbf(rbf(var) + 1e-5f))));
        const float mi = -m * inv;
        const __amdgpu_buffer_rsrc_t rs_u = __builtin_amdgcn_make_buffer_rsrc(u8, 0, L_ * D_ * 2, 0x00020000);
#pragma unroll
        for (int v = 0; v < 2; ++v) {
            float n[9];
#pragma unroll
            for (int i = 0; i < 9; ++i) n[i] = __builtin_fmaf(a[9 * v + i], inv, mi);
            char* wb = smem + KA_WB + (2 * wave + v) * 1024;
            *(u32x4*)(wb + lane * 16) = u32x4{pk_f16(n[0], n[1]), pk_f16(n[2], n[3]), pk_f16(n[4], n[5]), pk_f16(n[6], n[7])};
            const int uo = (blk * D_ + wave * 144 + 72 * v) * 8;
            __builtin_amdgcn_raw_buffer_store_b16((u16)pk_f16(n[8], n[8]), rs_u, (uo + 512 + lane) * 2, 0, SC1);
            __builtin_amdgcn_raw_buffer_store_b128(tr8x8(wb, lane), rs_u, (uo + lane * 8) * 2, 0, SC1);
        }
        STAMP0(2);
#pragma unroll
        for (int i = 0; i < 5; ++i) { const int o = lane * 16 + i * 1024; if (o < (wave < 3 ? KCH : 1536)) *(u32x4*)(smem + KA_K + wave * KCH + o) = u32x4{0, 0, 0, 0}; }
        WAIT_V(0);
        u32 old = 0;
        if (lane == 0) old = __hip_atomic_fetch_add((LAS u32*)&cnt[1], 1u, __ATOMIC_RELAXED, __HIP_MEMORY_SCOPE_WORKGROUP);
        if (__builtin_amdgcn_readfirstlane(old) == 3u) { if (lane < 8) __hip_atomic_store(epg + 256 + lane * 256 + blk, tag, RLX_AGENT); STAMP(3); }
        __builtin_amdgcn_s_setprio(0);
        if (tid < 24) *(u32x4*)(ws + WS_CTL + blk * 384 + tid * 16) = u32x4{0, 0, 0, 0};
    } else if (!conv) {
        unsigned long long pc; u32 back, len;
        asm volatile("s_getpc_b64 %0\n.Lcode_here:\n\ts_mov_b32 %1, .Lcode_here-.Lcode_begin\n\ts_mov_b32 %2, .Lcode_end-.Lcode_begin" : "=s"(pc), "=s"(back), "=s"(len));
        u32 junk = 0;
        if (wave < 7) junk = *(const GAS u32*)(wave == 6 ? ws + WS_EP + 1024 + lane * 128 : (const char*)(pc - back) + min((u32)((r5 - 27) * 128 + tid - 256) * 128u, len));
        const unsigned long long t0 = __builtin_amdgcn_s_memrealtime();
        while (__builtin_amdgcn_s_memrealtime() - t0 < PFA_DELAY) __builtin_amdgcn_s_sleep(16);
        if (wave == 7) junk = *(const GAS u32*)(ws + min((size_t)lane << 19, WS_END));
        prefetch_pair<10>(lb + KA_JUNK, w1win, F_ * 2, r5 * 256 + tid - 256);
        asm volatile("" :: "v"(junk));
        if (PFA > 0) prefetch_lines<9, false>((const char*)w1 + xg * 1152, F_ * 2, 819 * 9, PFA * 1280, (r5 - 27) * 256 + tid - 256, 1280);
        WAIT_V(0);
        STAMP(12 + (wave & 1));
    } else {
#pragma unroll 1
        for (int it = 0; it < (wave >= 6 ? 2 : 1); ++it) {
            const int c = it ? 2 : ((wave >> 1) & 1);
            table_task(kp, c, wave & 1, smem, chp, lane);
            lds_arrive(cnt + 2 + c, lane);
            kp = kp2;
        }
        if (wave == 4) STAMP(5);
        if (wave == 6) STAMP(6);
    }
    if (!conv) { STAMP_DUMP(); return; }
    if (wave < 3) {
        lds_wait<1>(cnt + 2 + wave, 2u);
        STAMP0(7);
        const u32 kb = lb + KA_K + wave * KCH - 8;
        const u32 ta = lb + wave * TBL + i16 * 64 + ((g ^ ((-(i16 >> 2)) & 3)) << 4);
        const int r1 = min(2 * (i16 >> 2) + (i16 & 3), 8);
        const u32 t1 = lb + wave * TBL + (48 + r1) * 64 + ((g ^ ((-(r1 >> 2)) & 3)) << 4);
        const float dsr = chp[wave][1];
        u32x4 T[8], A0[8], A1[8], AX[8];
        f32x4 a0, a1, ax;
#pragma unroll
        for (int ks = 0; ks < 3; ++ks) { DS_R128(T[ks], ta, ks * TKS); DS_R128(A0[ks], ta, ks * TKS + 1024); DS_R128(A1[ks], t1, ks * TKS); DS_R128(AX[ks], ta, ks * TKS + 2048); }
        WAIT_L0();
#pragma unroll
        for (int ks = 3; ks < 6; ++ks) { DS_R128(T[ks], ta, ks * TKS); DS_R128(A0[ks], ta, ks * TKS + 1024); DS_R128(A1[ks], t1, ks * TKS); DS_R128(AX[ks], ta, ks * TKS + 2048); }
        MFMA_F16_Z(a0, A0[0], T[0]); MFMA_F16_Z(a1, A1[0], T[0]); MFMA_F16_Z(ax, T[0], AX[0]);
#pragma unroll
        for (int ks = 1; ks < 8; ++ks) {
            if (ks == 3) {
                WAIT_L0();
#pragma unroll
                for (int k2 = 6; k2 < 8; ++k2) { DS_R128(T[k2], ta, k2 * TKS); DS_R128(A0[k2], ta, k2 * TKS + 1024); DS_R128(A1[k2], t1, k2 * TKS); DS_R128(AX[k2], ta, k2 * TKS + 2048); }
            }
            if (ks == 6) WAIT_L0();
            MFMA_F16(a0, A0[ks], T[ks]); MFMA_F16(a1, A1[ks], T[ks]); MFMA_F16(ax, T[ks], AX[ks]);
        }
        asm volatile("s_nop 7\n\ts_nop 3" : "+v"(a0), "+v"(a1), "+v"(ax));
        if (lane == 0) a0[0] += dsr;
        const u32 pz = kb + (542 - 16 * i16 - 4 * g) * 2;
        put4<1120>(pz, a0[0], a0[1], a0[2], a0[3]);
        put4<1120>(pz - 512, a1[0], a1[0], a1[1], a1[2]);
        if (lane == 63) put4<1120>(kb + (542 - 512) * 2, a1[2], a1[2], a1[2], 0.f);
        const int ph = i16 < 8 ? (i16 & 3) : 4 + (i16 & 1), a = i16 < 8 ? (i16 >> 2) : ((i16 - 8) >> 1);
        put4<160>(kb + 2240 + ph * 320 + (62 - 16 * a - 4 * g) * 2, ax[0], ax[1], ax[2], ax[3]);
        lds_arrive(cnt + 5, lane);
        if (wave == 2) STAMP(8);
    } else if (wave == 3) {
        const __amdgpu_buffer_rsrc_t rs_f = __builtin_amdgcn_make_buffer_rsrc(ep + 256 + xg * 256, 0, 1024, 0x00020000);
        u32 go[3];
#pragma unroll
        for (int i = 0; i < 3; ++i) { const int e = 64 * i + lane; go[i] = (e / 3) * (D_ * 16) + (e % 3) * 16 + 4096 - 1024 * i; PIN(go[i]); }
        const char* src = (const char*)(u8 + (size_t)d0 * 8) - 4096;
        const unsigned long long t0 = __builtin_amdgcn_s_memrealtime();
        __builtin_amdgcn_s_sleep(POLL1_FIRST);
        for (;;) {
            const u32x4 f = __builtin_amdgcn_raw_buffer_load_b128(rs_f, lane * 16, 0, SC1);
            if (__all(f[0] == tag && f[1] == tag && f[2] == tag && f[3] == tag)) break;
            if (__builtin_amdgcn_s_memrealtime() - t0 > SPIN_LIMIT) break;
            __builtin_amdgcn_s_sleep(POLL1_SLEEP);
        }
#pragma unroll
        for (int i = 0; i < 4; ++i) GLDS3(" sc1", lb + KA_U + 1536 + i * 3072, src + (size_t)i * (64 * D_ * 16), go[0], go[1], go[2]);
        STAMP(9);
        if (blk == 0 && lane == 0) __hip_atomic_store(epg, tag, RLX_AGENT);
        lds_wait<1>(cnt + 5, 3u);
    }
    f32x4* red = (f32x4*)(smem + KA_RED);
    char* ob = smem + KA_OUT;
    if (wave < 4) {
        f32x4 acc[8];
        const char* ka = smem + KA_K + (i16 & 1) * 1120 + (542 - 16 - (i16 & 14) + 8 * g) * 2;
        const u32 ub = lb + KA_U + (30 + 2 * i16 + g) * 48;
        B8 X, Y;
        if (wave < 3) {
            u32x4 A[5][2], A16, A32, T2[2][3], T3[4];
            const char* kc = ka + wave * KCH;
#pragma unroll
            for (int jd = 0; jd < 5; ++jd) { A[jd][0] = ld4(kc - 64 * jd); A[jd][1] = ld4(kc - 64 * (jd + 8)); }
            A16 = ld4(kc - 64 * 16);
            const char* tc = smem + KA_K + wave * KCH + 2240 + (g >> 1) * 320 + (i16 & 1) * 160 + (62 - (i16 & 14) + 8 * (g & 1)) * 2;
#pragma unroll
            for (int b = 0; b < 3; ++b) { T2[0][b] = ld4(tc - 32 * b); T2[1][b] = ld4(tc + 640 - 32 * b); }
#pragma unroll
            for (int b = 0; b < 4; ++b) T3[b] = ld4(tc + 1280 - 32 * b);
            {
                const u32 k1024 = *(const u16*)(smem + KA_K + wave * KCH + 2240 + 3 * 320 + 31 * 2);
#pragma unroll
                for (int w = 0; w < 4; ++w) {
                    const int e = 8 * g + 2 * w - i16;
                    A32[w] = ((e == 12 || e == 13) ? k1024 : 0u) | ((e == 11 || e == 12) ? (k1024 << 16) : 0u);
                }
            }
            const u32 ba = ub + 16 * wave;
            const int q = i16 >> 2, p = lane & 3;
            const u32 sa2 = lb + wave * SCH + (g >> 1) * 3088 + (128 * (g & 1) + 16 * q + 4 * p) * 2;
            const u32 sa3 = lb + wave * SCH + 6176 + (g >> 1) * 2048 + (128 * (g & 1) + 16 * q + 4 * p) * 2;
#pragma unroll
            for (int jd = 0; jd < 5; ++jd) { asm volatile("" : "+v"(A[jd][0]), "+v"(A[jd][1])); }
            asm volatile("" : "+v"(A16), "+v"(A32), "+v"(T3[0]), "+v"(T3[1]), "+v"(T3[2]), "+v"(T3[3]));
            asm volatile("" : "+v"(T2[0][0]), "+v"(T2[0][1]), "+v"(T2[0][2]), "+v"(T2[1][0]), "+v"(T2[1][1]), "+v"(T2[1][2]));
            BAR_L0();
            STAMP0(10); CSTAMP0(30);
#pragma unroll
            for (int m = 0; m < 8; ++m) DS_R128(X.f[m], ba, 1536 * m);
            WAIT_L0();
            cv_dense<1, true>(acc, A[0][0], A[0][1], X, Y, ba - 192);
#pragma unroll
            for (int m = 0; m < 6; ++m) MFMA_F16(acc[m + 2], A16, X.f[m]);
#pragma unroll
            for (int m = 0; m < 4; ++m) MFMA_F16(acc[m + 4], A32, X.f[m]);
            WAIT_L0();
            cv_dense<1>(acc, A[1][0], A[1][1], Y, X, ba - 384); WAIT_L0();
            cv_dense<1>(acc, A[2][0], A[2][1], X, Y, ba - 576); WAIT_L0();
            cv_dense<1>(acc, A[3][0], A[3][1], Y, X, ba - 768);
            BAR_L0();
            cv_dense<2>(acc, A[4][0], A[4][1], X, Y, sa2); WAIT_L0();
            {
                u32x2 h[8][2];
                int cnt = 0;
#pragma unroll
                for (int b = 0; b < 3; ++b)
#pragma unroll
                    for (int k = 0; k < 8; ++k) {
                        const int pr = k >> 2, be = k & 3;
                        MFMA_F16(acc[2 + be + b], Y.f[2 * be + pr], T2[pr][b]);
                        if (cnt < 4) { DS_TR(h[cnt][0], sa2, 512 * (4 + (cnt >> 1)) + 16 * (1 - (cnt & 1))); DS_TR(h[cnt][1], sa2, 512 * (4 + (cnt >> 1)) + 128 + 16 * (1 - (cnt & 1))); }
                        else if (cnt < 8) { DS_TR(h[cnt][0], sa3, 512 * (cnt - 4)); DS_TR(h[cnt][1], sa3, 512 * (cnt - 4) + 128); }
                        ++cnt;
                    }
#pragma unroll
                for (int k = 0; k < 8; ++k) X.f[k] = u32x4{h[k][0][0], h[k][0][1], h[k][1][0], h[k][1][1]};
            }
            WAIT_L0();
            MFMA_F16(acc[6], X.f[0], T2[0][0]); MFMA_F16(acc[7], X.f[2], T2[0][0]);
            MFMA_F16(acc[4], X.f[4], T3[0]); MFMA_F16(acc[5], X.f[5], T3[0]);
            MFMA_F16(acc[6], X.f[1], T2[1][0]); MFMA_F16(acc[7], X.f[3], T2[1][0]);
            MFMA_F16(acc[5], X.f[4], T3[1]);
            MFMA_F16(acc[6], X.f[6], T3[0]); MFMA_F16(acc[7], X.f[0], T2[0][1]);
            MFMA_F16(acc[6], X.f[5], T3[1]); MFMA_F16(acc[7], X.f[1], T2[1][1]);
            MFMA_F16(acc[6], X.f[4], T3[2]); MFMA_F16(acc[7], X.f[7], T3[0]);
            MFMA_F16(acc[7], X.f[6], T3[1]); MFMA_F16(acc[7], X.f[5], T3[2]); MFMA_F16(acc[7], X.f[4], T3[3]);
            FENCE8(acc);
            STAMP0(11); CSTAMP0(31);
            BAR_L0();
            const float scale = chp[wave][0];
#pragma unroll
            for (int t = 0; t < 8; ++t) {
                const f32x4 v = (acc[t] + red[(wave * 8 + t) * 64 + lane]) * scale;
                *(u32x2*)(ob + (32 * t + 2 * i16 + (g >> 1)) * 48 + wave * 16 + (g & 1) * 8) = u32x2{pk_bf16(v[0], v[1]), pk_bf16(v[2], v[3])};
            }
        } else {
            u32x4 A[3][3][2];
            f32x4 acc2[8];
#pragma unroll
            for (int c = 0; c < 3; ++c)
#pragma unroll
                for (int k = 0; k < 3; ++k) { A[c][k][0] = ld4(ka + c * KCH - 64 * (5 + k)); A[c][k][1] = ld4(ka + c * KCH - 64 * (13 + k)); }
#pragma unroll
            for (int c = 0; c < 3; ++c) asm volatile("" : "+v"(A[c][0][0]), "+v"(A[c][0][1]), "+v"(A[c][1][0]), "+v"(A[c][1][1]), "+v"(A[c][2][0]), "+v"(A[c][2][1]));
            const u32 ba = ub - 192 * 5, ra = lb + KA_RED + lane * 16;
            STAMP(19);
            WAIT_V(0);
            STAMP(20);
            BAR_L0();
#pragma unroll
            for (int m = 0; m < 8; ++m) DS_R128(X.f[m], ba, 1536 * m);
            WAIT_L0();
            cv_dense<1, true>(acc, A[0][0][0], A[0][0][1], X, Y, ba - 192); WAIT_L0();
            cv_dense<1>(acc, A[0][1][0], A[0][1][1], Y, X, ba - 384); WAIT_L0();
            cv_dense<1>(acc, A[0][2][0], A[0][2][1], X, Y, ba + 16); WAIT_L0();
            cv_dense<1, true, 0>(acc2, A[1][0][0], A[1][0][1], Y, X, ba + 16 - 192, acc, ra);
            BAR_L0();
            cv_dense<1>(acc2, A[1][1][0], A[1][1][1], X, Y, ba + 16 - 384); WAIT_L0();
            cv_dense<1>(acc2, A[1][2][0], A[1][2][1], Y, X, ba + 32); WAIT_L0();
            cv_dense<1, true, 1>(acc, A[2][0][0], A[2][0][1], X, Y, ba + 32 - 192, acc2, ra); WAIT_L0();
            cv_dense<1>(acc, A[2][1][0], A[2][1][1], Y, X, ba + 32 - 384); WAIT_L0();
            cv_dense<0>(acc, A[2][2][0], A[2][2][1], X, Y, 0);
            asm volatile("s_nop 1");
#pragma unroll
            for (int t = 0; t < 8; ++t) DS_W128(ra, acc[t], (16 + t) * 1024);
            STAMP(16);
            BAR_L0();
        }
    } else {
        BAR_L0();
        auto pool = [&](int c, int n, bool far) {
            const char* up = smem + KA_U + (29 + n) * 48 + 16 * c;
            u32 w[16], od[14], t2[14], v4[13];
#pragma unroll
            for (int k = 0; k < 4; ++k) { const u32x4 v = *(const u32x4*)(up + 48 * k); w[4 * k] = v[0]; w[4 * k + 1] = v[1]; w[4 * k + 2] = v[2]; w[4 * k + 3] = v[3]; }
#pragma unroll
            for (int k = 2; k < 14; ++k) od[k] = __builtin_amdgcn_alignbit(w[k + 1], w[k], 16);
#pragma unroll
            for (int k = 3; k < 14; ++k) t2[k] = pk_add_h(w[k], od[k - 1]);
#pragma unroll
            for (int k = 4; k < 13; ++k) v4[k] = pk_add_h(t2[k], t2[k - 1]);
            u32x4 s1, s2, so, se;
#pragma unroll
            for (int o = 0; o < 4; ++o) {
                s1[o] = pk_add_h(od[10 + o], t2[10 + o]);
                s2[o] = pk_add_h(v4[9 + o], w[7 + o]);
                so[o] = pk_add_h(pk_add_h(v4[9 + o], t2[7 + o]), od[9 + o]);
                se[o] = pk_add_h(pk_add_h(v4[6 + o], v4[4 + o]), w[2 + o]);
            }
            char* sp = smem + c * SCH;
            *(u32x4*)(sp + 16 + n * 16) = s1;
            *(u32x4*)(sp + 3088 + 16 + n * 16) = s2;
            if (far) { *(u32x4*)(sp + 6176 + n * 16) = so; *(u32x4*)(sp + 8224 + n * 16) = se; }
        };
        if (wave < 7) { pool(wave - 4, lane, true); pool(wave - 4, 64 + lane, true); }
        else {
            pool(0, 128 + lane, false); pool(1, 128 + lane, false); pool(2, 128 + lane, false);
            if (lane < 6) *(u32x4*)(smem + (lane >> 1) * SCH + (lane & 1) * 3088) = u32x4{0, 0, 0, 0};
        }
        if (wave == 4) STAMP(17);
        BAR_L0();
        prefetch_pair<10>(lb + KA_JUNK, w1win, F_ * 2, r5 * 256 + tid - 256);
        if (wave == 6) STAMP(18);
        BAR_L0();
    }
    BAR_L0();
#pragma unroll
    for (int it = 0; it < 2; ++it) {
        const int e = tid + 512 * it, n = e / 3, ch = e - 3 * n;
        if (e < 768) *(u32x4*)(y8 + ((size_t)n * D_ + d0 + ch) * 8) = *(const u32x4*)(ob + e * 16);
    }
    STAMP0(15);
    WAIT_V(0);
    STAMP_DUMP();
}

DEV f32x2 gelu2(f32x2 v) {
    const float k1 = -2.0f * 0.7978845608028654f * 1.4426950408889634f;
    const f32x2 t = v * __builtin_elementwise_fma(v * v, f32x2{0.044715f * k1, 0.044715f * k1}, f32x2{k1, k1});
    const f32x2 e = f32x2{__builtin_amdgcn_exp2f(t[0]), __builtin_amdgcn_exp2f(t[1])} + 1.0f;
    return v * f32x2{__builtin_amdgcn_rcpf(e[0]), __builtin_amdgcn_rcpf(e[1])};
}
#define W_POL ""
struct G1Set { u32x4 W[4]; u32x4 H[3]; };
template <int SLOT, int KS, bool RD>
DEV void g1_half(f32x4 (&acc)[4][3], const G1Set& cur, G1Set& nxt, const u32 (&wa)[4][2], u32 ha) {
    constexpr int WO = SLOT * 16384 + KS * 8192, HO = SLOT * 12288 + KS * 6144;
    u32x2 w[4][2];
#pragma unroll
    for (int i = 0; i < 12; ++i) {
        MFMA_BF(acc[i / 3][i % 3], cur.W[i / 3], cur.H[i % 3]);
        if (RD) {
            if (i == 0) { DS_R128(nxt.H[0], ha, HO); DS_TR(w[0][0], wa[0][0], WO); }
            if (i == 1) { DS_TR(w[0][1], wa[0][1], WO); DS_R128(nxt.H[1], ha, HO + 256); }
            if (i == 2) { DS_R128(nxt.H[2], ha, HO + 512); }
            if (i == 3) { DS_TR(w[1][0], wa[1][0], WO); DS_TR(w[1][1], wa[1][1], WO); }
            if (i == 4) { DS_TR(w[2][0], wa[2][0], WO); DS_TR(w[2][1], wa[2][1], WO); }
            if (i == 5) { DS_TR(w[3][0], wa[3][0], WO); DS_TR(w[3][1], wa[3][1], WO); }
        }
    }
    if (RD) {
#pragma unroll
        for (int t = 0; t < 4; ++t) nxt.W[t] = u32x4{w[t][0][0], w[t][0][1], w[t][1][0], w[t][1][1]};
    }
}

constexpr int G2_GS = 18432;
constexpr int G2_LDS = 65536 + 4 * G2_GS;
constexpr int SLAB = 9216;
constexpr int KB_JUNK = G2_LDS + 512;
constexpr int KB_LDS = KB_JUNK + 256;
struct G2Set { u32x4 W[2]; u32x4 A[9]; };
template <int SLOT, int KS, bool RD>
DEV void g2_half(f32x4 (&acc)[9][2], const G2Set& cur, G2Set& nxt, const u32 (&wa)[2][2], u32 ga0, u32 ga2) {
    constexpr int WO = SLOT * 16384 + KS * 8192, GO = (SLOT & 1) * G2_GS + KS * 9216;
    const u32 ga = SLOT >= 2 ? ga2 : ga0;
    u32x2 w[2][2];
#pragma unroll
    for (int i = 0; i < 18; ++i) {
        MFMA_BF(acc[i / 2][i % 2], cur.A[i / 2], cur.W[i % 2]);
        if (RD) {
            if (i == 0) DS_R128(nxt.A[0], ga, GO);
            if (i == 1) { DS_TR(w[0][0], wa[0][0], WO); DS_TR(w[0][1], wa[0][1], WO); }
            if (i == 2) { DS_TR(w[1][0], wa[1][0], WO); DS_TR(w[1][1], wa[1][1], WO); }
            if (i >= 3 && i <= 10) DS_R128(nxt.A[i - 2], ga, GO + (i - 2) * 256);
        }
    }
    if (RD) {
#pragma unroll
        for (int t = 0; t < 2; ++t) nxt.W[t] = u32x4{w[t][0][0], w[t][0][1], w[t][1][0], w[t][1][1]};
    }
}
#define PFM 11264
#define W2_POL ""
__global__ __launch_bounds__(512, 2) void k_back(const u16* __restrict__ x, const u16* __restrict__ w1, const u16* __restrict__ b1,
                                                 const u16* __restrict__ w2, const u16* __restrict__ b2, u16* __restrict__ out, char* ws STAMP_ARG) {
    extern __shared__ __attribute__((aligned(16))) char smem[];
    const u16* y8 = (const u16*)(ws + WS_Y);
    u16* h = (u16*)(ws + WS_H); u16* hh8 = (u16*)(ws + WS_HH); u16* G8 = (u16*)(ws + WS_G);
    char* slabs = ws + WS_SLAB; u32* ctl = (u32*)(ws + WS_CTL);
    constexpr int ST_FIRST = 32;
    const int lane = threadIdx.x & 63;
    const int wave8 = __builtin_amdgcn_readfirstlane(threadIdx.x >> 6);
    const int wave = wave8 & 3;
    STAMP_INIT();
    asm volatile("" :: "s"(x), "s"(w1), "s"(b1), "s"(w2), "s"(b2), "s"(out), "s"(ws));
    const int xg = blockIdx.x & 7, r5 = blockIdx.x >> 3;
    const u32 lbase = lds_addr(smem);
    const int g = lane >> 4, q = (lane & 15) >> 2, p = lane & 3, i16 = lane & 15;
    GAS u32* ctlg = (GAS u32*)ctl;
    const __amdgpu_buffer_rsrc_t rs_h = __builtin_amdgcn_make_buffer_rsrc(h, 0, L_ * D_ * 2, 0x00020000);
    if (wave8 < 4) {
        const int blk = blockIdx.x, rr = lane & 7, j = lane >> 3;
        const int ro = (blk * 8 + rr) * D_ + wave * 144;
        const int uo = (blk * D_ + wave * 144) * 8;
        u32x4 yu[2], xv[2]; u16 y9[2], x9[2];
#pragma unroll
        for (int v = 0; v < 2; ++v) {
            yu[v] = *(const u32x4*)(y8 + uo + 576 * v + lane * 8);
            xv[v] = __builtin_nontemporal_load((const u32x4*)(x + ro + 72 * v + j * 8));
            y9[v] = y8[uo + 576 * v + 512 + lane]; x9[v] = __builtin_nontemporal_load(x + ro + 72 * v + 64 + j);
        }
        char* wb = smem + 131072 + wave * 2048;
        float a[18];
#pragma unroll
        for (int v = 0; v < 2; ++v) {
            *(u32x4*)(wb + v * 1024 + lane * 16) = yu[v];
            const u32x4 yv = tr8x8(wb + v * 1024, lane);
            u32x4 hv;
#pragma unroll
            for (int i = 0; i < 4; ++i) {
                hv[i] = pk_bf16(bf_lo(xv[v][i]) + bf_lo(yv[i]), bf_hi(xv[v][i]) + bf_hi(yv[i]));
                a[9 * v + 2 * i] = bf_lo(hv[i]); a[9 * v + 2 * i + 1] = bf_hi(hv[i]);
            }
            a[9 * v + 8] = rbf(bf2f(x9[v]) + bf2f(y9[v]));
            char* hb = smem + 120832 + (rr * D_ + wave * 144 + 72 * v) * 2;
            *(u32x4*)(hb + j * 16) = hv;
            *(u16*)(hb + 128 + j * 2) = (u16)(__float_as_uint(a[9 * v + 8]) >> 16);
        }
        STAMP0(33);
        float mean;
        const float var = row_stats(a, (f32x2*)(smem + 139264), wave, lane, mean, [&]() { BAR_L0(); });
        const float r1 = __builtin_amdgcn_rsqf(var + 1e-5f);
        const float inv = r1 * __builtin_amdgcn_rsqf(var * r1 * r1 + 1e-5f);
        const float mi = -mean * inv;
        const __amdgpu_buffer_rsrc_t rs_hh = __builtin_amdgcn_make_buffer_rsrc(hh8, 0, L_ * D_ * 2, 0x00020000);
#pragma unroll
        for (int v = 0; v < 2; ++v) {
            float n[9];
#pragma unroll
            for (int i = 0; i < 9; ++i) n[i] = __builtin_fmaf(a[9 * v + i], inv, mi);
            *(u32x4*)(wb + v * 1024 + lane * 16) = u32x4{pk_bf16(n[0], n[1]), pk_bf16(n[2], n[3]), pk_bf16(n[4], n[5]), pk_bf16(n[6], n[7])};
            __builtin_amdgcn_raw_buffer_store_b16((u16)pk_bf16(n[8], n[8]), rs_hh, (uo + 576 * v + 512 + lane) * 2, 0, SC1);
            __builtin_amdgcn_raw_buffer_store_b128(tr8x8(wb + v * 1024, lane), rs_hh, (uo + 576 * v + lane * 8) * 2, 0, SC1);
        }
#pragma unroll
        for (int i = 0; i < 3; ++i) {
            const int o = (i * 256 + (int)threadIdx.x) * 16;
            if (i < 2 || wave == 0) __builtin_amdgcn_raw_buffer_store_b128(*(const u32x4*)(smem + 120832 + o), rs_h, blk * 9216 + o, 0, SC1);
        }
        STAMP0(34);
        WAIT_V(0);
        BAR();
        if (wave8 == 0 && lane < 32) __hip_atomic_fetch_add(ctlg + CTL_E3 + (lane * 8 + (blk >> 5)) * 32, 1u, RLX_AGENT);
        STAMP0(35);
    }
    const int kq = xg >> 1, tile = (xg & 1) * 32 + r5;
    const int s0 = (tile >> 2) * 128, d2 = (tile & 3) * 144;
    const char* w2win = (const char*)w2 + (size_t)kq * (1152 * L_ * 2) + (xg & 1) * 2048;
    const bool g1 = r5 < 27;
    const int id = xg * 27 + r5;
    const int fg = id / 6, mt1 = id % 6;
    const int f0 = fg * 128, d0 = mt1 * 96;
    if (wave8 >= 4) {
        u32 woff[4];
        const char* w2src = (const char*)(w2 + s0) + (size_t)kq * (1152 * L_ * 2) - 4096;
        auto woffs = [&](int pitch) {
#pragma unroll
            for (int i = 0; i < 4; ++i) {
                int row = 16 * wave + 4 * i + (lane >> 4), c = lane & 15;
                int swz = ((row & 3) << 2) | ((row >> 2) & 3);
                woff[i] = row * pitch + ((c ^ swz) << 4) + 4096 - 1024 * i;
            }
        };
        auto issue_w = [&](const char* ws, int slot) { GLDS4(W_POL, lbase + slot * 16384 + 4096 * wave, ws, woff[0], woff[1], woff[2], woff[3]); };
        auto issue_w2 = [&](const char* ws, int slot) { GLDS4(W2_POL, lbase + slot * 16384 + 4096 * wave, ws, woff[0], woff[1], woff[2], woff[3]); };
        if (g1) {
            u32 hoff[3];
            woffs(F_ * 2);
#pragma unroll
            for (int i = 0; i < 3; ++i) {
                int un = 192 * wave + 64 * i + lane;
                hoff[i] = (un / 96) * (D_ * 16) + (un % 96) * 16 + 4096 - 1024 * i;
            }
            const char* wsrc = (const char*)(w1 + f0) - 4096;
            const char* hsrc = (const char*)(hh8 + (size_t)d0 * 8) - 4096;
            auto issue_h = [&](int kt) { GLDS3(" sc1", lbase + 65536 + (kt & 3) * 12288 + 3072 * wave, hsrc + (size_t)kt * (8 * D_ * 16), hoff[0], hoff[1], hoff[2]); };
            auto issue = [&](int kt) { issue_w(wsrc + (size_t)kt * (64 * F_ * 2), kt & 3); issue_h(kt); };
#pragma unroll
            for (int kt = 0; kt < 2; ++kt) issue_w(wsrc + (size_t)kt * (64 * F_ * 2), kt);
            BAR();
            BAR();
#pragma unroll
            for (int kt = 2; kt < 4; ++kt) issue_w(wsrc + (size_t)kt * (64 * F_ * 2), kt);
            BAR();
            issue_h(0); issue_h(1); issue_h(2); issue_h(3);
            WAIT_V(9); BAR();
            WAIT_V(6); BAR(); issue(4);
            WAIT_V(10); BAR(); issue(5);
            for (int kt = 3; kt < 29; ++kt) {
                WAIT_V(14);
                BAR();
                issue(kt + 3);
            }
            woffs(L_ * 2);
            WAIT_V(14); BAR(); issue_w2(w2src, 0);
            WAIT_V(11); BAR(); issue_w2(w2src + 1 * (64 * L_ * 2), 1);
            WAIT_V(8); BAR(); issue_w2(w2src + 2 * (64 * L_ * 2), 2);
            BAR(); issue_w2(w2src + 3 * (64 * L_ * 2), 3);
        } else {
            woffs(L_ * 2);
            const int t = (r5 - 27) * 256 + (threadIdx.x - 256);
            constexpr int P0 = 819 * 9 + PFA * 1280;
            if (PFB > 0) prefetch_lines<9, false>((const char*)w1 + xg * 1152, F_ * 2, P0, 1280, t, 1280);
            BAR();
            if (PFB > 1) prefetch_lines<9, false>((const char*)w1 + xg * 1152, F_ * 2, P0 + 1280, 1280, t, 1280);
            BAR();
            prefetch_lines<9, false>((const char*)w1 + xg * 1152, F_ * 2, P0 + PFB * 1280, 18432 - P0 - PFB * 1280, t, 1280);
            if (wave8 == 4) STAMP(49);
            prefetch_lines<16, false>(w2win, L_ * 2, 0, 4096, t, 1280);
            if (wave8 == 4) STAMP(48);
            prefetch_lines<16, false>(w2win, L_ * 2, 4096 + PFM, 18432 - 4096 - PFM, t, 1280);
            if (wave8 == 4) STAMP(50);
#pragma unroll
            for (int kt = 0; kt < 4; ++kt) issue_w2(w2src + (size_t)kt * (64 * L_ * 2), kt);
        }
        if (wave8 == 4) STAMP(51);
        u32 goff[5];
#pragma unroll
        for (int i = 0; i < 5; ++i) {
            int un = min(64 * i + lane, 287);
            goff[i] = (2 * wave + un / 144) * (D_ * 16) + (un % 144) * 16 + 4096 - 1024 * (i & 3);
        }
        const char* gsrc = (const char*)(G8 + (size_t)d2 * 8) + (size_t)kq * (144 * D_ * 16) - 4096;
        auto issue_g = [&](int kt) {
            const char* gs = gsrc + (size_t)kt * (8 * D_ * 16);
            const u32 gd = lbase + 65536 + (kt & 3) * G2_GS + 4608 * wave;
            GLDS4(" sc1", gd, gs, goff[0], goff[1], goff[2], goff[3]);
            if (lane < 32) GLDS1(" sc1", gd + 4096, gs, goff[4]);
        };
        auto issue2 = [&](int kt) { issue_w2(w2src + (size_t)kt * (64 * L_ * 2), kt & 3); issue_g(kt); };
        if (wave8 == 4) {
            const unsigned long long t0 = __builtin_amdgcn_s_memrealtime();
            GAS u32* c = ctlg + CTL_E4 + (((kq * 4 + (tile & 3)) * 4 + ((tile >> 2) & 3)) * 4 + (lane & 3)) * 32;
            for (;;) {
                int v = (int)__hip_atomic_load(c, RLX_AGENT);
                v += __builtin_amdgcn_update_dpp(0, v, 0xB1, 0xf, 0xf, true);
                v += __builtin_amdgcn_update_dpp(0, v, 0x4E, 0xf, 0xf, true);
                if (__builtin_amdgcn_readfirstlane(v) >= 54) break;
                if (__builtin_amdgcn_s_memrealtime() - t0 > SPIN_LIMIT) break;
                __builtin_amdgcn_s_sleep(2);
            }
        }
        asm volatile("" ::: "memory");
        BAR();
        issue_g(0); issue_g(1); issue_g(2); issue_g(3);
        WAIT_V(15); BAR();
        WAIT_V(10); BAR(); issue2(4);
        WAIT_V(14); BAR(); issue2(5);
        for (int kt = 3; kt < 15; ++kt) {
            WAIT_V(18);
            BAR();
            issue2(kt + 3);
        }
        WAIT_V(18); BAR();
        WAIT_V(9); BAR();
        WAIT_V(0); BAR();
    } else if (g1) {
        const int wf = wave >> 1, wd = wave & 1;
        u32 wa[4][2];
#pragma unroll
        for (int t = 0; t < 4; ++t)
#pragma unroll
            for (int tau = 0; tau < 2; ++tau) {
                int row = 8 * g + 4 * tau + q;
                int swz = (q << 2) | ((2 * g + tau) & 3);
                int ch = 2 * (4 * wf + t) + (p >> 1);
                wa[t][tau] = lbase + row * 256 + ((ch ^ swz) << 4) + 8 * (p & 1);
            }
        const u32 ha = lbase + 65536 + g * 1536 + (48 * wd + i16) * 16;
        f32x4 acc[4][3];
#pragma unroll
        for (int t = 0; t < 4; ++t) {
            const u32x2 bw = *(const u32x2*)(b1 + f0 + 64 * wf + 16 * t + 4 * g);
#pragma unroll
            for (int m = 0; m < 3; ++m) { acc[t][m] = f32x4{bf_lo(bw[0]), bf_hi(bw[0]), bf_lo(bw[1]), bf_hi(bw[1])}; PIN(acc[t][m]); }
        }
        G1Set X, Y;
#pragma unroll
        for (int t = 0; t < 4; ++t) { Y.W[t] = u32x4{0, 0, 0, 0}; asm volatile("" : "+v"(Y.W[t])); }
#pragma unroll
        for (int m = 0; m < 3; ++m) { Y.H[m] = u32x4{0, 0, 0, 0}; asm volatile("" : "+v"(Y.H[m])); }
        if (wave8 == 0) {
            const unsigned long long t0 = __builtin_amdgcn_s_memrealtime();
            GAS u32* c = ctlg + CTL_E3 + ((blockIdx.x & 31) * 8 + (lane & 7)) * 32;
            for (;;) {
                u32 v = __hip_atomic_load(c, RLX_AGENT);
                if (__all(v >= 32u)) break;
                if (__builtin_amdgcn_s_memrealtime() - t0 > SPIN_LIMIT) break;
                __builtin_amdgcn_s_sleep(1);
            }
        }
        STAMP0(36);
        asm volatile("" ::: "memory");
        BAR();
        asm volatile("s_nop 1");
        for (int kt = 0; kt < 32; kt += 4) {
            BAR_L0();
            if (kt == 0) { STAMP0(37); CSTAMP0(60); }
            g1_half<0, 0, true>(acc, Y, X, wa, ha); WAIT_L0();
            g1_half<0, 1, true>(acc, X, Y, wa, ha);
            BAR_L0();
            g1_half<1, 0, true>(acc, Y, X, wa, ha); WAIT_L0();
            g1_half<1, 1, true>(acc, X, Y, wa, ha);
            BAR_L0();
            g1_half<2, 0, true>(acc, Y, X, wa, ha); WAIT_L0();
            g1_half<2, 1, true>(acc, X, Y, wa, ha);
            BAR_L0();
            g1_half<3, 0, true>(acc, Y, X, wa, ha); WAIT_L0();
            g1_half<3, 1, true>(acc, X, Y, wa, ha);
        }
        BAR_L0();
        g1_half<0, 0, false>(acc, Y, X, wa, ha);
        FENCE6(acc[2][0], acc[2][1], acc[2][2], acc[3][0], acc[3][1], acc[3][2]);
#pragma unroll
        for (int t = 0; t < 2; ++t)
#pragma unroll
            for (int m = 0; m < 3; ++m) PIN(acc[t][m]);
        STAMP0(38); CSTAMP0(61);
        const __amdgpu_buffer_rsrc_t rs_g = __builtin_amdgcn_make_buffer_rsrc(G8, 0, D_ * F_ * 2, 0x00020000);
        const int go = (((f0 + 64 * wf) / 8 + 2 * (g & 1) + (g >> 1)) * D_ + d0 + 48 * wd + i16) * 16;
#pragma unroll
        for (int t = 0; t < 4; t += 2) {
#pragma unroll
            for (int m = 0; m < 3; ++m) {
                u32 o[2][2];
#pragma unroll
                for (int k = 0; k < 2; ++k) {
                    const f32x4 v = acc[t + k][m];
                    const f32x2 lo = gelu2(f32x2{v[0], v[1]}), hi = gelu2(f32x2{v[2], v[3]});
                    o[k][0] = pk_bf16(lo[0], lo[1]);
                    o[k][1] = pk_bf16(hi[0], hi[1]);
                }
                auto r0 = __builtin_amdgcn_permlane16_swap(o[0][0], o[1][0], false, false);
                auto r1 = __builtin_amdgcn_permlane16_swap(o[0][1], o[1][1], false, false);
                __builtin_amdgcn_raw_buffer_store_b128(u32x4{r0[0], r1[0], r0[1], r1[1]}, rs_g, go + (2 * t * D_ + 16 * m) * 16, 0, SC1);
            }
        }
        STAMP0(39);
        WAIT_V(0);
        if (lane < 4) __hip_atomic_fetch_add(ctlg + CTL_E4 + ((((fg / 9) * 4 + (2 * mt1 + wd) / 3) * 4 + lane) * 4 + (fg & 3)) * 32, 1u, RLX_AGENT);
        STAMP0(40);
    }
    const __amdgpu_buffer_rsrc_t rs_s = __builtin_amdgcn_make_buffer_rsrc(slabs + (size_t)tile * (16 * SLAB), 0, 16 * SLAB, 0x00020000);
    GAS u32* fl = ctlg + CTL_XF + tile * 128;
    const int s = s0 + 32 * kq + i16;
    u32x2 hv[2][2];
    float bb[2];
    auto load_res = [&]() {
#pragma unroll
        for (int j = 0; j < 2; ++j)
#pragma unroll
            for (int t = 0; t < 2; ++t)
                hv[j][t] = __builtin_amdgcn_raw_buffer_load_b64(rs_h, ((s + 16 * t) * D_ + d2 + 16 * (j ? 8 : wave8) + 4 * g) * 2, 0, SC1);
        bb[0] = bf2f(b2[s]); bb[1] = bf2f(b2[s + 16]);
    };
    if (wave8 < 4) {
        if (g1) {
            constexpr int PW = (PFM + 107) / 108;
            prefetch_pair<16>(lbase + KB_JUNK, w2win, L_ * 2, 4096 + min((r5 * 4 + wave) * PW + lane, PFM - 1));
            if (lane < PW - 64) prefetch_pair<16>(lbase + KB_JUNK, w2win, L_ * 2, 4096 + min((r5 * 4 + wave) * PW + 64 + lane, PFM - 1));
        }
        u32 wa[2][2];
#pragma unroll
        for (int t = 0; t < 2; ++t)
#pragma unroll
            for (int tau = 0; tau < 2; ++tau) {
                int row = 8 * g + 4 * tau + q;
                int swz = (q << 2) | ((2 * g + tau) & 3);
                int ch = 2 * (2 * wave + t) + (p >> 1);
                wa[t][tau] = lbase + row * 256 + ((ch ^ swz) << 4) + 8 * (p & 1);
            }
        const u32 ga0 = lbase + 65536 + g * 2304 + i16 * 16, ga2 = ga0 + 2 * G2_GS;
        f32x4 acc[9][2];
#pragma unroll
        for (int m = 0; m < 9; ++m)
#pragma unroll
            for (int t = 0; t < 2; ++t) { acc[m][t] = f32x4{0, 0, 0, 0}; asm volatile("" : "+v"(acc[m][t])); }
        G2Set X, Y;
#pragma unroll
        for (int t = 0; t < 2; ++t) { Y.W[t] = u32x4{0, 0, 0, 0}; asm volatile("" : "+v"(Y.W[t])); }
#pragma unroll
        for (int m = 0; m < 9; ++m) { Y.A[m] = u32x4{0, 0, 0, 0}; asm volatile("" : "+v"(Y.A[m])); }
        asm volatile("" ::: "memory");
        BAR();
        asm volatile("" ::: "memory");
        STAMP0(41);
        load_res();
        asm volatile("s_nop 1");
        for (int kt = 0; kt < 16; kt += 4) {
            BAR_L0();
            if (kt == 0) { STAMP0(42); CSTAMP0(62); }
            g2_half<0, 0, true>(acc, Y, X, wa, ga0, ga2); WAIT_L0();
            g2_half<0, 1, true>(acc, X, Y, wa, ga0, ga2);
            BAR_L0();
            g2_half<1, 0, true>(acc, Y, X, wa, ga0, ga2); WAIT_L0();
            g2_half<1, 1, true>(acc, X, Y, wa, ga0, ga2);
            BAR_L0();
            g2_half<2, 0, true>(acc, Y, X, wa, ga0, ga2); WAIT_L0();
            g2_half<2, 1, true>(acc, X, Y, wa, ga0, ga2);
            BAR_L0();
            g2_half<3, 0, true>(acc, Y, X, wa, ga0, ga2); WAIT_L0();
            g2_half<3, 1, true>(acc, X, Y, wa, ga0, ga2);
        }
        BAR_L0();
        g2_half<0, 0, true>(acc, Y, X, wa, ga0, ga2); WAIT_L0();
        g2_half<0, 1, true>(acc, X, Y, wa, ga0, ga2);
        BAR_L0();
        g2_half<1, 0, true>(acc, Y, X, wa, ga0, ga2); WAIT_L0();
        g2_half<1, 1, true>(acc, X, Y, wa, ga0, ga2);
        WAIT_L0();
        g2_half<0, 0, false>(acc, Y, X, wa, ga0, ga2);
        FENCE6(acc[6][0], acc[6][1], acc[7][0], acc[7][1], acc[8][0], acc[8][1]);
#pragma unroll
        for (int m = 0; m < 6; ++m)
#pragma unroll
            for (int t = 0; t < 2; ++t) PIN(acc[m][t]);
        STAMP0(43); CSTAMP0(63);
        if (wave == kq) {
            f32x4* own = (f32x4*)(smem + 32768);
#pragma unroll
            for (int i = 0; i < 18; ++i) own[i * 64 + lane] = acc[i >> 1][i & 1];
        } else {
            const int so = (wave * 4 + kq) * SLAB + lane * 16;
#pragma unroll
            for (int m = 0; m < 9; ++m)
                __builtin_amdgcn_raw_buffer_store_b128(u32x4{pk_bf16(acc[m][0][0], acc[m][0][1]), pk_bf16(acc[m][0][2], acc[m][0][3]),
                                                             pk_bf16(acc[m][1][0], acc[m][1][1]), pk_bf16(acc[m][1][2], acc[m][1][3])}, rs_s, so + m * 1024, 0, SC1);
            WAIT_V(0);
            if (lane == 0) __hip_atomic_store(fl + wave * 32 + kq, 1u, RLX_AGENT);
        }
        if (wave == ((kq + 1) & 3)) STAMP(44);
    } else {
        load_res();
        if (wave8 == 4) {
            const unsigned long long t0 = __builtin_amdgcn_s_memrealtime();
            for (;;) {
                u32 f = __hip_atomic_load(fl + kq * 32 + (lane & 3), RLX_AGENT);
                if (__all(f == 1u || (lane & 3) == kq)) break;
                if (__builtin_amdgcn_s_memrealtime() - t0 > SPIN_LIMIT) break;
                __builtin_amdgcn_s_sleep(2);
            }
        }
    }
    BAR_L0();
    STAMP0(45);
    u32x4 part[2][3];
#pragma unroll
    for (int j = 0; j < 2; ++j) {
        const int po = j == 0 ? wave8 * 1024 + lane * 16 : (wave8 == 0 ? 8192 + lane * 16 : 0x40000000);
#pragma unroll
        for (int k = 0; k < 3; ++k) part[j][k] = __builtin_amdgcn_raw_buffer_load_b128(rs_s, (kq * 4 + ((kq + 1 + k) & 3)) * SLAB + po, 0, SC1);
    }
#pragma unroll
    for (int j = 0; j < 2; ++j) {
        if (j == 1 && wave8 != 0) break;
        const int m = j ? 8 : wave8;
#pragma unroll
        for (int t = 0; t < 2; ++t) {
            f32x4 v = ((const f32x4*)(smem + 32768))[(m * 2 + t) * 64 + lane];
#pragma unroll
            for (int k = 0; k < 3; ++k) v += f32x4{bf_lo(part[j][k][2 * t]), bf_hi(part[j][k][2 * t]), bf_lo(part[j][k][2 * t + 1]), bf_hi(part[j][k][2 * t + 1])};
            const u32 y0 = pk_bf16(v[0] + bb[t], v[1] + bb[t]), y1 = pk_bf16(v[2] + bb[t], v[3] + bb[t]);
            *(u32x2*)(out + (size_t)(s + 16 * t) * D_ + d2 + 16 * m + 4 * g) =
                u32x2{pk_bf16(bf_lo(hv[j][t][0]) + bf_lo(y0), bf_hi(hv[j][t][0]) + bf_hi(y0)), pk_bf16(bf_lo(hv[j][t][1]) + bf_lo(y1), bf_hi(hv[j][t][1]) + bf_hi(y1))};
        }
    }
    STAMP0(47);
    STAMP_DUMP();
    asm volatile(".Lcode_end:");
}

extern "C" void kernel_launch(void* const* d_in, const int* in_sizes, int n_in,
                              void* d_out, int out_size, void* d_ws, size_t ws_size,
                              hipStream_t stream) {
    const u16* x = (const u16*)d_in[0];
    const u16* logA = (const u16*)d_in[1];
    const u16* Aim = (const u16*)d_in[2];
    const u16* Cre = (const u16*)d_in[3];
    const u16* Cim = (const u16*)d_in[4];
    const u16* logdt = (const u16*)d_in[5];
    const u16* Dskip = (const u16*)d_in[6];
    const u16* w1 = (const u16*)d_in[8];
    const u16* b1 = (const u16*)d_in[9];
    const u16* w2 = (const u16*)d_in[10];
    const u16* b2 = (const u16*)d_in[11];
    char* ws = (char*)d_ws;
    (void)hipFuncSetAttribute((const void*)k_front, hipFuncAttributeMaxDynamicSharedMemorySize, LDS_REQ(KA_LDS));
    (void)hipFuncSetAttribute((const void*)k_back, hipFuncAttributeMaxDynamicSharedMemorySize, LDS_REQ(KB_LDS));
    k_front<<<256, 512, LDS_REQ(KA_LDS), stream>>>(x, ws, logA, Aim, Cre, Cim, logdt, Dskip, w1 STAMP_PASS);
    k_back<<<256, 512, LDS_REQ(KB_LDS), stream>>>(x, w1, b1, w2, b2, (u16*)d_out, ws STAMP_PASS);
}
```

```cpp
#define DIAG 2
#include <hip/hip_runtime.h>
#include <stdint.h>

typedef unsigned short u16;
typedef unsigned int u32;
typedef short s16x4 __attribute__((ext_vector_type(4)));
typedef short s16x8 __attribute__((ext_vector_type(8)));
typedef __bf16 bf16x8 __attribute__((ext_vector_type(8)));
typedef __bf16 bf16x2 __attribute__((ext_vector_type(2)));
typedef _Float16 f16x8 __attribute__((ext_vector_type(8)));
typedef _Float16 f16x2 __attribute__((ext_vector_type(2)));
typedef float f32x4 __attribute__((ext_vector_type(4)));
typedef float f32x2 __attribute__((ext_vector_type(2)));
typedef u32 u32x4 __attribute__((ext_vector_type(4)));
typedef u32 u32x2 __attribute__((ext_vector_type(2)));
#define LAS __attribute__((address_space(3)))
#define DEV __device__ __forceinline__

constexpr int L_ = 2048, D_ = 576, N_ = 128, F_ = 4608;

#define STAMP(id)
#define CSTAMP(id)
#define STAMP0(id) do { if ((id) == 37) tq37 = __builtin_amdgcn_s_memrealtime(); if ((id) == 38) tq38 = __builtin_amdgcn_s_memrealtime(); if ((id) == 42) tq42 = __builtin_amdgcn_s_memrealtime(); } while (0)
#define CSTAMP0(id)
#define STAMP_INIT()
#define STAMP_DUMP()
#define STAMP_ARG
#define STAMP_PASS
#define LDS_REQ(n) (n)

DEV float bf_lo(u32 w) { return __uint_as_float(w << 16); }
DEV float bf_hi(u32 w) { return __uint_as_float(w & 0xffff0000u); }
DEV float bf2f(u16 v) { return __uint_as_float(((u32)v) << 16); }
DEV u32 pk_bf16(float a, float b) { f32x2 v = {a, b}; return __builtin_bit_cast(u32, __builtin_convertvector(v, bf16x2)); }
DEV u32 pk_f16(float a, float b) { f32x2 v = {a, b}; return __builtin_bit_cast(u32, __builtin_convertvector(v, f16x2)); }
DEV float rbf(float a) { return bf_lo(pk_bf16(a, a)); }
DEV float h_lo(u32 w) { return (float)__builtin_bit_cast(f16x2, w)[0]; }
DEV float h_hi(u32 w) { return (float)__builtin_bit_cast(f16x2, w)[1]; }

DEV u32 lds_addr(const void* p) { return (u32)(uintptr_t)(LAS const char*)p; }
template <int CTRL> DEV float dpp_get(float v) { return __builtin_bit_cast(float, __builtin_amdgcn_update_dpp(0, __builtin_bit_cast(int, v), CTRL, 0xf, 0xf, true)); }
DEV float swap32_add(float v) {
    auto r = __builtin_amdgcn_permlane32_swap(__float_as_uint(v), __float_as_uint(v), false, false);
    return __uint_as_float(r[0]) + __uint_as_float(r[1]);
}
DEV float swap16_add(float v) {
    auto r = __builtin_amdgcn_permlane16_swap(__float_as_uint(v), __float_as_uint(v), false, false);
    return __uint_as_float(r[0]) + __uint_as_float(r[1]);
}
DEV float sum_j(float v) { return swap32_add(swap16_add(v + dpp_get<0x128>(v))); }
DEV float sum_j4(float v) { return swap16_add(v + dpp_get<0x128>(v)); }
DEV u32 umax(u32 a, u32 b) { return a > b ? a : b; }
template <int CTRL> DEV u32 dpp_umax(u32 v) { return umax(v, (u32)__builtin_amdgcn_update_dpp(0, (int)v, CTRL, 0xf, 0xf, true)); }
DEV u32 wave_umax(u32 v) {
    v = dpp_umax<0xB1>(v); v = dpp_umax<0x4E>(v); v = dpp_umax<0x141>(v); v = dpp_umax<0x140>(v);
    { auto r = __builtin_amdgcn_permlane16_swap(v, v, false, false); v = umax(r[0], r[1]); }
    { auto r = __builtin_amdgcn_permlane32_swap(v, v, false, false); v = umax(r[0], r[1]); }
    return v;
}
DEV u32x4 tr8x8(const char* wbuf, int lane) {
    const int q = (lane & 15) >> 2, p = lane & 3;
    const char* a = wbuf + ((lane >> 4) * 16 + (p >> 1) * 8 + q) * 16 + (p & 1) * 8;
    s16x4 lo = __builtin_amdgcn_ds_read_tr16_b64_v4i16((LAS s16x4*)a);
    s16x4 hi = __builtin_amdgcn_ds_read_tr16_b64_v4i16((LAS s16x4*)(a + 64));
    return __builtin_bit_cast(u32x4, __builtin_shufflevector(lo, hi, 0, 1, 2, 3, 4, 5, 6, 7));
}

#define MFMA_BF(acc, a, b) asm volatile("v_mfma_f32_16x16x32_bf16 %0, %1, %2, %0" : "+v"(acc) : "v"(a), "v"(b))
#define MFMA_F16(acc, a, b) asm volatile("v_mfma_f32_16x16x32_f16 %0, %1, %2, %0" : "+v"(acc) : "v"(a), "v"(b))
#define DS_R128(dst, addr, off) asm volatile("ds_read_b128 %0, %1 offset:%2" : "=v"(dst) : "v"(addr), "i"(off))
#define DS_TR(dst, addr, off) asm volatile("ds_read_b64_tr_b16 %0, %1 offset:%2" : "=v"(dst) : "v"(addr), "i"(off))
#define WAIT_L0() asm volatile("s_waitcnt lgkmcnt(0)" ::: "memory")
#define WAIT_V(n) asm volatile("s_waitcnt vmcnt(" #n ")" ::: "memory")
#define BAR_L0() asm volatile("s_waitcnt lgkmcnt(0)\n\ts_barrier" ::: "memory")
#define BAR() asm volatile("s_barrier" ::: "memory")
#define PIN(x) asm volatile("" : "+v"(x))
#define FENCE6(a0, a1, a2, a3, a4, a5) asm volatile("s_nop 7\n\ts_nop 3" : "+v"(a0), "+v"(a1), "+v"(a2), "+v"(a3), "+v"(a4), "+v"(a5) :: "memory")
#define FENCE8(a) asm volatile("s_nop 7\n\ts_nop 3" : "+v"(a[0]), "+v"(a[1]), "+v"(a[2]), "+v"(a[3]), "+v"(a[4]), "+v"(a[5]), "+v"(a[6]), "+v"(a[7]) :: "memory")
#define GLDS4(POL, lds, base, o0, o1, o2, o3) asm volatile("s_mov_b32 m0, %0\n\ts_nop 0\n\t" \
    "global_load_lds_dwordx4 %2, %1" POL "\n\tglobal_load_lds_dwordx4 %3, %1 offset:1024" POL "\n\t" \
    "global_load_lds_dwordx4 %4, %1 offset:2048" POL "\n\tglobal_load_lds_dwordx4 %5, %1 offset:3072" POL \
    :: "s"(lds), "s"(base), "v"(o0), "v"(o1), "v"(o2), "v"(o3) : "memory")
#define GLDS3(POL, lds, base, o0, o1, o2) asm volatile("s_mov_b32 m0, %0\n\ts_nop 0\n\t" \
    "global_load_lds_dwordx4 %2, %1" POL "\n\tglobal_load_lds_dwordx4 %3, %1 offset:1024" POL "\n\t" \
    "global_load_lds_dwordx4 %4, %1 offset:2048" POL \
    :: "s"(lds), "s"(base), "v"(o0), "v"(o1), "v"(o2) : "memory")
#define GLDS1(POL, lds, base, o0) asm volatile("s_mov_b32 m0, %0\n\ts_nop 0\n\t" \
    "global_load_lds_dwordx4 %2, %1" POL :: "s"(lds), "s"(base), "v"(o0) : "memory")

#define GAS __attribute__((address_space(1)))
#define RLX_AGENT __ATOMIC_RELAXED, __HIP_MEMORY_SCOPE_AGENT
constexpr int SC1 = 16;
constexpr unsigned long long SPIN_LIMIT = 2000000ull;
constexpr int CTL_E3 = 0, CTL_E4 = 8192, CTL_XF = 16384;

constexpr size_t SZ = (size_t)L_ * D_ * 2;
constexpr size_t WS_CTL = 0, WS_EP = 98304, WS_U = 131072, WS_Y = WS_U + SZ, WS_HH = WS_Y + SZ, WS_H = WS_HH + SZ, WS_G = WS_H + SZ,
                 WS_SLAB = WS_G + (size_t)D_ * F_ * 2, WS_END = WS_SLAB + (size_t)64 * 16 * 9216;
static_assert(WS_END < (64u << 19), "the translation warm-up covers 64 x 512 KB");

template <int LPR, bool NT>
DEV void prefetch_lines(const char* base, u32 pitch, int first, int count, int t, int stride) {
    u32 prev = 0;
    for (int ln = t; ln < count; ln += stride) {
        const u32 l = first + ln;
        const u32* a = (const u32*)(base + (size_t)(l / LPR) * pitch + (l % LPR) * 128);
        const u32 v = NT ? __builtin_nontemporal_load(a) : *a;
        asm volatile("" :: "v"(prev));
        prev = v;
    }
    asm volatile("" :: "v"(prev));
}

template <int LPR>
DEV void prefetch_pair(u32 lds_junk, const char* base, u32 pitch, u32 l) {
    const u32 off = (l / LPR) * pitch + (l % LPR) * 128;
    asm volatile("s_mov_b32 m0, %0\n\ts_nop 0\n\tglobal_load_lds_dword %1, %2 nt\n\tglobal_load_lds_dword %1, %2" :: "s"(lds_junk), "v"(off), "s"(base) : "memory");
}
DEV void lds_arrive(volatile LAS u32* c, int lane) { WAIT_L0(); if (lane == 0) __hip_atomic_fetch_add((LAS u32*)c, 1u, __ATOMIC_RELAXED, __HIP_MEMORY_SCOPE_WORKGROUP); }
template <int SLEEP> DEV void lds_wait(volatile LAS u32* c, u32 n) { while (*c < n) { if (SLEEP) __builtin_amdgcn_s_sleep(SLEEP); } asm volatile("" ::: "memory"); }

template <class SYNC>
DEV float row_stats(const float (&a)[18], f32x2* red, int wave, int lane, float& mean_out, SYNC sync) {
    float s0 = a[0] + a[1], s1 = a[2] + a[3];
#pragma unroll
    for (int i = 4; i < 18; i += 2) { s0 += a[i]; s1 += a[i + 1]; }
    const float mw = sum_j(s0 + s1) * (1.0f / 144);
    float q0 = 0, q1 = 0;
#pragma unroll
    for (int i = 0; i < 18; i += 2) { const float d0 = a[i] - mw, d1 = a[i + 1] - mw; q0 = __builtin_fmaf(d0, d0, q0); q1 = __builtin_fmaf(d1, d1, q1); }
    const float q = sum_j(q0 + q1);
    if (lane < 8) red[wave * 8 + lane] = f32x2{mw, q};
    sync();
    const f32x2 pj = red[lane & 31];
    const float mean = sum_j4(pj[0]) * 0.25f;
    const float dj = pj[0] - mean;
    const float m2 = sum_j4(__builtin_fmaf(144.0f * dj, dj, pj[1]));
    mean_out = mean;
    return m2 * (1.0f / D_);
}

typedef f32x2 cf;
DEV cf cmul(cf p, cf q) {
    cf t, r;
    asm("v_pk_mul_f32 %0, %1, %2 op_sel:[0,0] op_sel_hi:[0,1]" : "=v"(t) : "v"(p), "v"(q));
    asm("v_pk_fma_f32 %0, %1, %2, %3 op_sel:[1,1,0] op_sel_hi:[1,0,1] neg_lo:[1,0,0]" : "=v"(r) : "v"(p), "v"(q), "v"(t));
    return r;
}
DEV u32 hmul(u32 p, u32 q) {
    u32 t, r;
    asm("v_pk_mul_f16 %0, %1, %2 op_sel:[0,0] op_sel_hi:[0,1]" : "=v"(t) : "v"(p), "v"(q));
    asm("v_pk_fma_f16 %0, %1, %2, %3 op_sel:[1,1,0] op_sel_hi:[1,0,1] neg_lo:[1,0,0]" : "=v"(r) : "v"(p), "v"(q), "v"(t));
    return r;
}
DEV u32 toh(cf v) { return pk_f16(v[0], v[1]); }
DEV cf cexp_s(float zr2, float zi, float s) {
    const float mag = __builtin_amdgcn_exp2f(zr2 * s);
    const float th = zi * s;
    const float c_hi = 0.15915494309189535f, c_lo = 6.4206382e-9f;
    const float hi = th * c_hi;
    const float lo = __builtin_fmaf(th, c_hi, -hi) + th * c_lo;
    const float rev = __builtin_amdgcn_fractf(hi) + lo;
    return cf{mag * __builtin_amdgcn_cosf(rev), mag * __builtin_amdgcn_sinf(rev)};
}
constexpr int TKS = 57 * 64;
constexpr int TBL = 8 * TKS;
constexpr int KA_K = 3 * TBL;
constexpr int KCH = 4160;
constexpr int KA_U = KA_K + 3 * KCH;
constexpr int KA_WB = KA_U + 288 * 48;
constexpr int KA_MISC = KA_WB + 8192;
constexpr int KA_JUNK = KA_MISC + 512;
constexpr int KA_LDS = KA_JUNK + 256;
constexpr int SCH = 2 * 3088 + 2 * 2048;
constexpr int KA_RED = 3 * SCH, KA_OUT = KA_RED + 24576;
static_assert(KA_OUT + 12288 <= KA_K && KA_K % 16 == 0 && KA_RED % 16 == 0, "LDS map");

DEV u32 pk_add_h(u32 a, u32 b) { return __builtin_bit_cast(u32, __builtin_bit_cast(f16x2, a) + __builtin_bit_cast(f16x2, b)); }
DEV u32x4 ld4(const char* p) { const u32* w = (const u32*)p; return u32x4{w[0], w[1], w[2], w[3]}; }
template <int CP>
DEV void put4(u32 p, float v0, float v1, float v2, float v3) {
    const u32 p32 = pk_f16(v3, v2), p10 = pk_f16(v1, v0), p21 = pk_f16(v2, v1);
    asm volatile("ds_write_b64 %0, %1 offset:%4\n\tds_write_b16 %0, %5 offset:2\n\tds_write_b32 %0, %2 offset:4\n\tds_write_b16_d16_hi %0, %3 offset:8"
                 :: "v"(p), "v"(u32x2{p32, p10}), "v"(p21), "v"(p10), "i"(CP + 4), "v"(p32) : "memory");
}
struct B8 { u32x4 f[8]; };
#define MFMA_F16_Z(acc, a, b) asm volatile("v_mfma_f32_16x16x32_f16 %0, %1, %2, 0" : "=&v"(acc) : "v"(a), "v"(b))
#define DS_W128(addr, src, off) asm volatile("ds_write_b128 %0, %1 offset:%2" :: "v"(addr), "v"(src), "i"(off) : "memory")
template <int MODE, bool ZERO = false, int FL = -1>
DEV void cv_dense(f32x4 (&acc)[8], const u32x4& A0, const u32x4& A1, const B8& cur, B8& nxt, u32 ba, const f32x4* old = nullptr, u32 ra = 0) {
    u32x2 h[8][2];
#pragma unroll
    for (int k = 0; k < 15; ++k) {
        if (k < 8) { if (ZERO) MFMA_F16_Z(acc[k], A0, cur.f[k]); else MFMA_F16(acc[k], A0, cur.f[k]); }
        else MFMA_F16(acc[k - 7], A1, cur.f[k - 8]);
        if (MODE == 1 && k < 8) DS_R128(nxt.f[k], ba, 1536 * k);
        if (MODE == 2 && k < 8) { DS_TR(h[k][0], ba, 512 * (k >> 1) + 16 * (1 - (k & 1))); DS_TR(h[k][1], ba, 512 * (k >> 1) + 128 + 16 * (1 - (k & 1))); }
        if (FL >= 0 && k >= 7) DS_W128(ra, old[k - 7], (FL * 8 + k - 7) * 1024);
    }
    if (MODE == 2) {
#pragma unroll
        for (int k = 0; k < 8; ++k) nxt.f[k] = u32x4{h[k][0][0], h[k][0][1], h[k][1][0], h[k][1][1]};
    }
}
struct KParam { u32 la, ai, cr, ci; u16 dt, ds; };
DEV KParam kparam_load(const u16* logA, const u16* Aim, const u16* Cre, const u16* Cim, const u16* logdt, const u16* Dskip, int d, int lane) {
    const int o = d * N_ + 2 * lane;
    return KParam{__builtin_nontemporal_load((const u32*)(logA + o)), __builtin_nontemporal_load((const u32*)(Aim + o)),
                  __builtin_nontemporal_load((const u32*)(Cre + o)), __builtin_nontemporal_load((const u32*)(Cim + o)),
                  __builtin_nontemporal_load(logdt + d), __builtin_nontemporal_load(Dskip + d)};
}
DEV void table_task(const KParam& k, int c, int hf, char* smem, f32x2* chp, int lane) {
    const int sh = hf ? 0 : 16, i16 = lane & 15;
    const float dt = rbf(__expf(bf2f(k.dt))), dsk = bf2f(k.ds);
    const float zr = -(rbf(__expf(bf_hi(k.la << sh))) * dt) * 1.4426950408889634f, zi = bf_hi(k.ai << sh) * dt;
    const float cs = __uint_as_float(wave_umax(umax(umax(k.cr & 0x7fff0000u, (k.cr << 16) & 0x7fff0000u), umax(k.ci & 0x7fff0000u, (k.ci << 16) & 0x7fff0000u))));
    const float ksc = fmaxf(fmaxf(2.0f * dt * cs, fabsf(dsk) * (1.0f / 1024)), 1e-30f);
    const float rs = __builtin_amdgcn_rcpf(ksc);
    const cf C = cf{bf_hi(k.cr << sh), bf_hi(k.ci << sh)} * (2.0f * dt * rs);
    char* tb = smem + c * TBL + (hf * 4 + (lane >> 4)) * TKS;
    auto put = [&](int row, u32 v) { *(u32*)(tb + row * 64 + ((i16 ^ (((-(row >> 2)) & 3) << 2)) << 2)) = v; };
    const cf e1 = cexp_s(zr, zi, 1.f), w1 = cexp_s(zr, -zi, 16.f), g1 = cexp_s(zr, zi, 256.f);
    const cf e2 = cmul(e1, e1), e3 = cmul(e2, e1), e4 = cmul(e2, e2);
    const cf w2 = cmul(w1, w1), w3 = cmul(w2, w1), w4 = cmul(w2, w2), w8 = cmul(w4, w4), w12 = cmul(w8, w4);
    const u32 f[4] = {0, toh(e1), toh(e2), toh(e3)};
    const u32 wf[4] = {0x00003c00u, toh(w1), toh(w2), toh(w3)}, wq[4] = {0, toh(w4), toh(w8), toh(w12)};
    u32 cq[5], gk[8];
    { cf t = C; cq[0] = toh(t);
#pragma unroll
      for (int a = 1; a < 5; ++a) { t = cmul(t, e4); cq[a] = toh(t); } }
    { cf t = g1; gk[1] = toh(t);
#pragma unroll
      for (int a = 2; a < 8; ++a) { t = cmul(t, g1); gk[a] = toh(t); } }
#pragma unroll
    for (int a = 0; a < 4; ++a)
#pragma unroll
        for (int b = 0; b < 4; ++b) {
            put(4 * a + b, a == 0 ? wf[b] : b == 0 ? wq[a] : hmul(wq[a], wf[b]));
            const u32 r0 = b == 0 ? cq[a] : hmul(cq[a], f[b]);
            put(16 + 4 * a + b, r0);
            if ((b & 1) == 0) put(48 + 2 * a + (b >> 1), hmul(r0, gk[1]));
        }
    put(56, hmul(cq[4], gk[1]));
#pragma unroll
    for (int i = 0; i < 4; ++i) {
        put(32 + i, hmul(cq[i + 1], gk[2])); put(36 + i, hmul(cq[i + 1], gk[3]));
        put(40 + 2 * i, hmul(cq[2], gk[4 + i])); put(41 + 2 * i, hmul(cq[4], gk[4 + i]));
    }
    if (lane == 0 && hf == 0) chp[c] = f32x2{ksc, dsk * rs};
}

#define POLL1_FIRST 26
#define POLL1_SLEEP 2
#define PFA 0
#define PFB 0
#define PFA_DELAY 330
__global__ __launch_bounds__(512, 2) void k_front(const u16* __restrict__ x, char* ws, const u16* __restrict__ logA, const u16* __restrict__ Aim,
                                                  const u16* __restrict__ Cre, const u16* __restrict__ Cim, const u16* __restrict__ logdt,
                                                  const u16* __restrict__ Dskip, const u16* __restrict__ w1 STAMP_ARG) {
    extern __shared__ __attribute__((aligned(16))) char smem[];
    constexpr int ST_FIRST = 0;
    asm volatile(".Lcode_begin:");
    unsigned long long tq37, tq38, tq42; const unsigned long long tqa = __builtin_amdgcn_s_memrealtime();
    const int tid = threadIdx.x, lane = tid & 63;
    const int wave8 = __builtin_amdgcn_readfirstlane(tid >> 6);
    const int wave = wave8;
    STAMP_INIT();
    asm volatile("" :: "s"(x), "s"(ws), "s"(logA), "s"(Aim), "s"(Cre), "s"(Cim), "s"(logdt), "s"(Dskip));
    const int blk = blockIdx.x, xg = blk & 7, r5 = blk >> 3;
    const bool conv = r5 < 27;
    const int d0 = conv ? (xg * 9 + r5 / 3) * 8 + min(3 * (r5 % 3), 5) : 0;
    u16* u8 = (u16*)(ws + WS_U);
    u16* y8 = (u16*)(ws + WS_Y);
    u32* ep = (u32*)(ws + WS_EP);
    GAS u32* epg = (GAS u32*)ep;
    const int rr = lane & 7, j8 = lane >> 3;
    u32x4 xv[2]; u16 x9[2]; u32 tag; KParam kp, kp2;
    if (wave < 4) {
        const int ro = (blk * 8 + rr) * D_ + wave * 144;
#pragma unroll
        for (int v = 0; v < 2; ++v) { xv[v] = __builtin_nontemporal_load((const u32x4*)(x + ro + 72 * v + j8 * 8)); x9[v] = __builtin_nontemporal_load(x + ro + 72 * v + 64 + j8); }
        tag = __hip_atomic_load(epg, RLX_AGENT) + 1u;
    } else {
        kp = kparam_load(logA, Aim, Cre, Cim, logdt, Dskip, d0 + ((wave >> 1) & 1), lane);
        kp2 = kparam_load(logA, Aim, Cre, Cim, logdt, Dskip, d0 + 2, lane);
    }
    volatile LAS u32* cnt = (volatile LAS u32*)(smem + KA_MISC + 256);
    if (tid < 6) cnt[tid] = 0;
    BAR_L0();
    f32x2* chp = (f32x2*)(smem + KA_MISC + 288);
    const int i16 = lane & 15, g = lane >> 4;
    const u32 lb = lds_addr(smem);
    const char* w1win = (const char*)w1 + ((27 * xg) / 6) * 256;
    if (wave < 4) {
        __builtin_amdgcn_s_setprio(2);
        float a[18];
#pragma unroll
        for (int v = 0; v < 2; ++v) {
#pragma unroll
            for (int i = 0; i < 4; ++i) { a[9 * v + 2 * i] = bf_lo(xv[v][i]); a[9 * v + 2 * i + 1] = bf_hi(xv[v][i]); }
            a[9 * v + 8] = bf2f(x9[v]);
        }
        PIN(a[0]); STAMP0(1);
        float mean;
        const float var = row_stats(a, (f32x2*)(smem + KA_MISC), wave, lane, mean, [&]() { lds_arrive(cnt, lane); lds_wait<0>(cnt, 4u); });
        const float m = rbf(mean);
        const float inv = __builtin_amdgcn_rcpf(rbf(__builtin_amdgcn_sqrtf(rbf(rbf(var) + 1e-5f))));
        const float mi = -m * inv;
        const __amdgpu_buffer_rsrc_t rs_u = __builtin_amdgcn_make_buffer_rsrc(u8, 0, L_ * D_ * 2, 0x00020000);
#pragma unroll
        for (int v = 0; v < 2; ++v) {
            float n[9];
#pragma unroll
            for (int i = 0; i < 9; ++i) n[i] = __builtin_fmaf(a[9 * v + i], inv, mi);
            char* wb = smem + KA_WB + (2 * wave + v) * 1024;
            *(u32x4*)(wb + lane * 16) = u32x4{pk_f16(n[0], n[1]), pk_f16(n[2], n[3]), pk_f16(n[4], n[5]), pk_f16(n[6], n[7])};
            const int uo = (blk * D_ + wave * 144 + 72 * v) * 8;
            __builtin_amdgcn_raw_buffer_store_b16((u16)pk_f16(n[8], n[8]), rs_u, (uo + 512 + lane) * 2, 0, SC1);
            __builtin_amdgcn_raw_buffer_store_b128(tr8x8(wb, lane), rs_u, (uo + lane * 8) * 2, 0, SC1);
        }
        STAMP0(2);
#pragma unroll
        for (int i = 0; i < 5; ++i) { const int o = lane * 16 + i * 1024; if (o < (wave < 3 ? KCH : 1536)) *(u32x4*)(smem + KA_K + wave * KCH + o) = u32x4{0, 0, 0, 0}; }
        WAIT_V(0);
        u32 old = 0;
        if (lane == 0) old = __hip_atomic_fetch_add((LAS u32*)&cnt[1], 1u, __ATOMIC_RELAXED, __HIP_MEMORY_SCOPE_WORKGROUP);
        if (__builtin_amdgcn_readfirstlane(old) == 3u) { if (lane < 8) __hip_atomic_store(epg + 256 + lane * 256 + blk, tag, RLX_AGENT); STAMP(3); }
        __builtin_amdgcn_s_setprio(0);
        if (tid < 24) *(u32x4*)(ws + WS_CTL + blk * 384 + tid * 16) = u32x4{0, 0, 0, 0};
    } else if (!conv) {
        unsigned long long pc; u32 back, len;
        asm volatile("s_getpc_b64 %0\n.Lcode_here:\n\ts_mov_b32 %1, .Lcode_here-.Lcode_begin\n\ts_mov_b32 %2, .Lcode_end-.Lcode_begin" : "=s"(pc), "=s"(back), "=s"(len));
        u32 junk = 0;
        if (wave < 7) junk = *(const GAS u32*)(wave == 6 ? ws + WS_EP + 1024 + lane * 128 : (const char*)(pc - back) + min((u32)((r5 - 27) * 128 + tid - 256) * 128u, len));
        const unsigned long long t0 = __builtin_amdgcn_s_memrealtime();
        while (__builtin_amdgcn_s_memrealtime() - t0 < PFA_DELAY) __builtin_amdgcn_s_sleep(16);
        if (wave == 7) junk = *(const GAS u32*)(ws + min((size_t)lane << 19, WS_END));
        prefetch_pair<10>(lb + KA_JUNK, w1win, F_ * 2, r5 * 256 + tid - 256);
        asm volatile("" :: "v"(junk));
        if (PFA > 0) prefetch_lines<9, false>((const char*)w1 + xg * 1152, F_ * 2, 819 * 9, PFA * 1280, (r5 - 27) * 256 + tid - 256, 1280);
        WAIT_V(0);
        STAMP(12 + (wave & 1));
    } else {
#pragma unroll 1
        for (int it = 0; it < (wave >= 6 ? 2 : 1); ++it) {
            const int c = it ? 2 : ((wave >> 1) & 1);
            table_task(kp, c, wave & 1, smem, chp, lane);
            lds_arrive(cnt + 2 + c, lane);
            kp = kp2;
        }
        if (wave == 4) STAMP(5);
        if (wave == 6) STAMP(6);
    }
    if (!conv) { STAMP_DUMP(); return; }
    if (wave < 3) {
        lds_wait<1>(cnt + 2 + wave, 2u);
        STAMP0(7);
        const u32 kb = lb + KA_K + wave * KCH - 8;
        const u32 ta = lb + wave * TBL + i16 * 64 + ((g ^ ((-(i16 >> 2)) & 3)) << 4);
        const int r1 = min(2 * (i16 >> 2) + (i16 & 3), 8);
        const u32 t1 = lb + wave * TBL + (48 + r1) * 64 + ((g ^ ((-(r1 >> 2)) & 3)) << 4);
        const float dsr = chp[wave][1];
        u32x4 T[8], A0[8], A1[8], AX[8];
        f32x4 a0, a1, ax;
#pragma unroll
        for (int ks = 0; ks < 3; ++ks) { DS_R128(T[ks], ta, ks * TKS); DS_R128(A0[ks], ta, ks * TKS + 1024); DS_R128(A1[ks], t1, ks * TKS); DS_R128(AX[ks], ta, ks * TKS + 2048); }
        WAIT_L0();
#pragma unroll
        for (int ks = 3; ks < 6; ++ks) { DS_R128(T[ks], ta, ks * TKS); DS_R128(A0[ks], ta, ks * TKS + 1024); DS_R128(A1[ks], t1, ks * TKS); DS_R128(AX[ks], ta, ks * TKS + 2048); }
        MFMA_F16_Z(a0, A0[0], T[0]); MFMA_F16_Z(a1, A1[0], T[0]); MFMA_F16_Z(ax, T[0], AX[0]);
#pragma unroll
        for (int ks = 1; ks < 8; ++ks) {
            if (ks == 3) {
                WAIT_L0();
#pragma unroll
                for (int k2 = 6; k2 < 8; ++k2) { DS_R128(T[k2], ta, k2 * TKS); DS_R128(A0[k2], ta, k2 * TKS + 1024); DS_R128(A1[k2], t1, k2 * TKS); DS_R128(AX[k2], ta, k2 * TKS + 2048); }
            }
            if (ks == 6) WAIT_L0();
            MFMA_F16(a0, A0[ks], T[ks]); MFMA_F16(a1, A1[ks], T[ks]); MFMA_F16(ax, T[ks], AX[ks]);
        }
        asm volatile("s_nop 7\n\ts_nop 3" : "+v"(a0), "+v"(a1), "+v"(ax));
        if (lane == 0) a0[0] += dsr;
        const u32 pz = kb + (542 - 16 * i16 - 4 * g) * 2;
        put4<1120>(pz, a0[0], a0[1], a0[2], a0[3]);
        put4<1120>(pz - 512, a1[0], a1[0], a1[1], a1[2]);
        if (lane == 63) put4<1120>(kb + (542 - 512) * 2, a1[2], a1[2], a1[2], 0.f);
        const int ph = i16 < 8 ? (i16 & 3) : 4 + (i16 & 1), a = i16 < 8 ? (i16 >> 2) : ((i16 - 8) >> 1);
        put4<160>(kb + 2240 + ph * 320 + (62 - 16 * a - 4 * g) * 2, ax[0], ax[1], ax[2], ax[3]);
        lds_arrive(cnt + 5, lane);
        if (wave == 2) STAMP(8);
    } else if (wave == 3) {
        const __amdgpu_buffer_rsrc_t rs_f = __builtin_amdgcn_make_buffer_rsrc(ep + 256 + xg * 256, 0, 1024, 0x00020000);
        u32 go[3];
#pragma unroll
        for (int i = 0; i < 3; ++i) { const int e = 64 * i + lane; go[i] = (e / 3) * (D_ * 16) + (e % 3) * 16 + 4096 - 1024 * i; PIN(go[i]); }
        const char* src = (const char*)(u8 + (size_t)d0 * 8) - 4096;
        const unsigned long long t0 = __builtin_amdgcn_s_memrealtime();
        __builtin_amdgcn_s_sleep(POLL1_FIRST);
        for (;;) {
            const u32x4 f = __builtin_amdgcn_raw_buffer_load_b128(rs_f, lane * 16, 0, SC1);
            if (__all(f[0] == tag && f[1] == tag && f[2] == tag && f[3] == tag)) break;
            if (__builtin_amdgcn_s_memrealtime() - t0 > SPIN_LIMIT) break;
            __builtin_amdgcn_s_sleep(POLL1_SLEEP);
        }
#pragma unroll
        for (int i = 0; i < 4; ++i) GLDS3(" sc1", lb + KA_U + 1536 + i * 3072, src + (size_t)i * (64 * D_ * 16), go[0], go[1], go[2]);
        STAMP(9);
        if (blk == 0 && lane == 0) __hip_atomic_store(epg, tag, RLX_AGENT);
        lds_wait<1>(cnt + 5, 3u);
    }
    f32x4* red = (f32x4*)(smem + KA_RED);
    char* ob = smem + KA_OUT;
    if (wave < 4) {
        f32x4 acc[8];
        const char* ka = smem + KA_K + (i16 & 1) * 1120 + (542 - 16 - (i16 & 14) + 8 * g) * 2;
        const u32 ub = lb + KA_U + (30 + 2 * i16 + g) * 48;
        B8 X, Y;
        if (wave < 3) {
            u32x4 A[5][2], A16, A32, T2[2][3], T3[4];
            const char* kc = ka + wave * KCH;
#pragma unroll
            for (int jd = 0; jd < 5; ++jd) { A[jd][0] = ld4(kc - 64 * jd); A[jd][1] = ld4(kc - 64 * (jd + 8)); }
            A16 = ld4(kc - 64 * 16);
            const char* tc = smem + KA_K + wave * KCH + 2240 + (g >> 1) * 320 + (i16 & 1) * 160 + (62 - (i16 & 14) + 8 * (g & 1)) * 2;
#pragma unroll
            for (int b = 0; b < 3; ++b) { T2[0][b] = ld4(tc - 32 * b); T2[1][b] = ld4(tc + 640 - 32 * b); }
#pragma unroll
            for (int b = 0; b < 4; ++b) T3[b] = ld4(tc + 1280 - 32 * b);
            {
                const u32 k1024 = *(const u16*)(smem + KA_K + wave * KCH + 2240 + 3 * 320 + 31 * 2);
#pragma unroll
                for (int w = 0; w < 4; ++w) {
                    const int e = 8 * g + 2 * w - i16;
                    A32[w] = ((e == 12 || e == 13) ? k1024 : 0u) | ((e == 11 || e == 12) ? (k1024 << 16) : 0u);
                }
            }
            const u32 ba = ub + 16 * wave;
            const int q = i16 >> 2, p = lane & 3;
            const u32 sa2 = lb + wave * SCH + (g >> 1) * 3088 + (128 * (g & 1) + 16 * q + 4 * p) * 2;
            const u32 sa3 = lb + wave * SCH + 6176 + (g >> 1) * 2048 + (128 * (g & 1) + 16 * q + 4 * p) * 2;
#pragma unroll
            for (int jd = 0; jd < 5; ++jd) { asm volatile("" : "+v"(A[jd][0]), "+v"(A[jd][1])); }
            asm volatile("" : "+v"(A16), "+v"(A32), "+v"(T3[0]), "+v"(T3[1]), "+v"(T3[2]), "+v"(T3[3]));
            asm volatile("" : "+v"(T2[0][0]), "+v"(T2[0][1]), "+v"(T2[0][2]), "+v"(T2[1][0]), "+v"(T2[1][1]), "+v"(T2[1][2]));
            BAR_L0();
            STAMP0(10); CSTAMP0(30);
#pragma unroll
            for (int m = 0; m < 8; ++m) DS_R128(X.f[m], ba, 1536 * m);
            WAIT_L0();
            cv_dense<1, true>(acc, A[0][0], A[0][1], X, Y, ba - 192);
#pragma unroll
            for (int m = 0; m < 6; ++m) MFMA_F16(acc[m + 2], A16, X.f[m]);
#pragma unroll
            for (int m = 0; m < 4; ++m) MFMA_F16(acc[m + 4], A32, X.f[m]);
            WAIT_L0();
            cv_dense<1>(acc, A[1][0], A[1][1], Y, X, ba - 384); WAIT_L0();
            cv_dense<1>(acc, A[2][0], A[2][1], X, Y, ba - 576); WAIT_L0();
            cv_dense<1>(acc, A[3][0], A[3][1], Y, X, ba - 768);
            BAR_L0();
            cv_dense<2>(acc, A[4][0], A[4][1], X, Y, sa2); WAIT_L0();
            {
                u32x2 h[8][2];
                int cnt = 0;
#pragma unroll
                for (int b = 0; b < 3; ++b)
#pragma unroll
                    for (int k = 0; k < 8; ++k) {
                        const int pr = k >> 2, be = k & 3;
                        MFMA_F16(acc[2 + be + b], Y.f[2 * be + pr], T2[pr][b]);
                        if (cnt < 4) { DS_TR(h[cnt][0], sa2, 512 * (4 + (cnt >> 1)) + 16 * (1 - (cnt & 1))); DS_TR(h[cnt][1], sa2, 512 * (4 + (cnt >> 1)) + 128 + 16 * (1 - (cnt & 1))); }
                        else if (cnt < 8) { DS_TR(h[cnt][0], sa3, 512 * (cnt - 4)); DS_TR(h[cnt][1], sa3, 512 * (cnt - 4) + 128); }
                        ++cnt;
                    }
#pragma unroll
                for (int k = 0; k < 8; ++k) X.f[k] = u32x4{h[k][0][0], h[k][0][1], h[k][1][0], h[k][1][1]};
            }
            WAIT_L0();
            MFMA_F16(acc[6], X.f[0], T2[0][0]); MFMA_F16(acc[7], X.f[2], T2[0][0]);
            MFMA_F16(acc[4], X.f[4], T3[0]); MFMA_F16(acc[5], X.f[5], T3[0]);
            MFMA_F16(acc[6], X.f[1], T2[1][0]); MFMA_F16(acc[7], X.f[3], T2[1][0]);
            MFMA_F16(acc[5], X.f[4], T3[1]);
            MFMA_F16(acc[6], X.f[6], T3[0]); MFMA_F16(acc[7], X.f[0], T2[0][1]);
            MFMA_F16(acc[6], X.f[5], T3[1]); MFMA_F16(acc[7], X.f[1], T2[1][1]);
            MFMA_F16(acc[6], X.f[4], T3[2]); MFMA_F16(acc[7], X.f[7], T3[0]);
            MFMA_F16(acc[7], X.f[6], T3[1]); MFMA_F16(acc[7], X.f[5], T3[2]); MFMA_F16(acc[7], X.f[4], T3[3]);
            FENCE8(acc);
            STAMP0(11); CSTAMP0(31);
            BAR_L0();
            const float scale = chp[wave][0];
#pragma unroll
            for (int t = 0; t < 8; ++t) {
                const f32x4 v = (acc[t] + red[(wave * 8 + t) * 64 + lane]) * scale;
                *(u32x2*)(ob + (32 * t + 2 * i16 + (g >> 1)) * 48 + wave * 16 + (g & 1) * 8) = u32x2{pk_bf16(v[0], v[1]), pk_bf16(v[2], v[3])};
            }
        } else {
            u32x4 A[3][3][2];
            f32x4 acc2[8];
#pragma unroll
            for (int c = 0; c < 3; ++c)
#pragma unroll
                for (int k = 0; k < 3; ++k) { A[c][k][0] = ld4(ka + c * KCH - 64 * (5 + k)); A[c][k][1] = ld4(ka + c * KCH - 64 * (13 + k)); }
#pragma unroll
            for (int c = 0; c < 3; ++c) asm volatile("" : "+v"(A[c][0][0]), "+v"(A[c][0][1]), "+v"(A[c][1][0]), "+v"(A[c][1][1]), "+v"(A[c][2][0]), "+v"(A[c][2][1]));
            const u32 ba = ub - 192 * 5, ra = lb + KA_RED + lane * 16;
            STAMP(19);
            WAIT_V(0);
            STAMP(20);
            BAR_L0();
#pragma unroll
            for (int m = 0; m < 8; ++m) DS_R128(X.f[m], ba, 1536 * m);
            WAIT_L0();
            cv_dense<1, true>(acc, A[0][0][0], A[0][0][1], X, Y, ba - 192); WAIT_L0();
            cv_dense<1>(acc, A[0][1][0], A[0][1][1], Y, X, ba - 384); WAIT_L0();
            cv_dense<1>(acc, A[0][2][0], A[0][2][1], X, Y, ba + 16); WAIT_L0();
            cv_dense<1, true, 0>(acc2, A[1][0][0], A[1][0][1], Y, X, ba + 16 - 192, acc, ra);
            BAR_L0();
            cv_dense<1>(acc2, A[1][1][0], A[1][1][1], X, Y, ba + 16 - 384); WAIT_L0();
            cv_dense<1>(acc2, A[1][2][0], A[1][2][1], Y, X, ba + 32); WAIT_L0();
            cv_dense<1, true, 1>(acc, A[2][0][0], A[2][0][1], X, Y, ba + 32 - 192, acc2, ra); WAIT_L0();
            cv_dense<1>(acc, A[2][1][0], A[2][1][1], Y, X, ba + 32 - 384); WAIT_L0();
            cv_dense<0>(acc, A[2][2][0], A[2][2][1], X, Y, 0);
            asm volatile("s_nop 1");
#pragma unroll
            for (int t = 0; t < 8; ++t) DS_W128(ra, acc[t], (16 + t) * 1024);
            STAMP(16);
            BAR_L0();
        }
    } else {
        BAR_L0();
        auto pool = [&](int c, int n, bool far) {
            const char* up = smem + KA_U + (29 + n) * 48 + 16 * c;
            u32 w[16], od[14], t2[14], v4[13];
#pragma unroll
            for (int k = 0; k < 4; ++k) { const u32x4 v = *(const u32x4*)(up + 48 * k); w[4 * k] = v[0]; w[4 * k + 1] = v[1]; w[4 * k + 2] = v[2]; w[4 * k + 3] = v[3]; }
#pragma unroll
            for (int k = 2; k < 14; ++k) od[k] = __builtin_amdgcn_alignbit(w[k + 1], w[k], 16);
#pragma unroll
            for (int k = 3; k < 14; ++k) t2[k] = pk_add_h(w[k], od[k - 1]);
#pragma unroll
            for (int k = 4; k < 13; ++k) v4[k] = pk_add_h(t2[k], t2[k - 1]);
            u32x4 s1, s2, so, se;
#pragma unroll
            for (int o = 0; o < 4; ++o) {
                s1[o] = pk_add_h(od[10 + o], t2[10 + o]);
                s2[o] = pk_add_h(v4[9 + o], w[7 + o]);
                so[o] = pk_add_h(pk_add_h(v4[9 + o], t2[7 + o]), od[9 + o]);
                se[o] = pk_add_h(pk_add_h(v4[6 + o], v4[4 + o]), w[2 + o]);
            }
            char* sp = smem + c * SCH;
            *(u32x4*)(sp + 16 + n * 16) = s1;
            *(u32x4*)(sp + 3088 + 16 + n * 16) = s2;
            if (far) { *(u32x4*)(sp + 6176 + n * 16) = so; *(u32x4*)(sp + 8224 + n * 16) = se; }
        };
        if (wave < 7) { pool(wave - 4, lane, true); pool(wave - 4, 64 + lane, true); }
        else {
            pool(0, 128 + lane, false); pool(1, 128 + lane, false); pool(2, 128 + lane, false);
            if (lane < 6) *(u32x4*)(smem + (lane >> 1) * SCH + (lane & 1) * 3088) = u32x4{0, 0, 0, 0};
        }
        if (wave == 4) STAMP(17);
        BAR_L0();
        prefetch_pair<10>(lb + KA_JUNK, w1win, F_ * 2, r5 * 256 + tid - 256);
        if (wave == 6) STAMP(18);
        BAR_L0();
    }
    BAR_L0();
#pragma unroll
    for (int it = 0; it < 2; ++it) {
        const int e = tid + 512 * it, n = e / 3, ch = e - 3 * n;
        if (e < 768) *(u32x4*)(y8 + ((size_t)n * D_ + d0 + ch) * 8) = *(const u32x4*)(ob + e * 16);
    }
    WAIT_V(0);
    if (blk == 8 && tid == 0) { unsigned long long* dg = (unsigned long long*)(ws + (100u << 20)); dg[0] = tqa; dg[1] = __builtin_amdgcn_s_memrealtime(); }
    STAMP_DUMP();
}

DEV f32x2 gelu2(f32x2 v) {
    const float k1 = -2.0f * 0.7978845608028654f * 1.4426950408889634f;
    const f32x2 t = v * __builtin_elementwise_fma(v * v, f32x2{0.044715f * k1, 0.044715f * k1}, f32x2{k1, k1});
    const f32x2 e = f32x2{__builtin_amdgcn_exp2f(t[0]), __builtin_amdgcn_exp2f(t[1])} + 1.0f;
    return v * f32x2{__builtin_amdgcn_rcpf(e[0]), __builtin_amdgcn_rcpf(e[1])};
}
#define W_POL ""
struct G1Set { u32x4 W[4]; u32x4 H[3]; };
template <int SLOT, int KS, bool RD>
DEV void g1_half(f32x4 (&acc)[4][3], const G1Set& cur, G1Set& nxt, const u32 (&wa)[4][2], u32 ha) {
    constexpr int WO = SLOT * 16384 + KS * 8192, HO = SLOT * 12288 + KS * 6144;
    u32x2 w[4][2];
#pragma unroll
    for (int i = 0; i < 12; ++i) {
        MFMA_BF(acc[i / 3][i % 3], cur.W[i / 3], cur.H[i % 3]);
        if (RD) {
            if (i == 0) { DS_R128(nxt.H[0], ha, HO); DS_TR(w[0][0], wa[0][0], WO); }
            if (i == 1) { DS_TR(w[0][1], wa[0][1], WO); DS_R128(nxt.H[1], ha, HO + 256); }
            if (i == 2) { DS_R128(nxt.H[2], ha, HO + 512); }
            if (i == 3) { DS_TR(w[1][0], wa[1][0], WO); DS_TR(w[1][1], wa[1][1], WO); }
            if (i == 4) { DS_TR(w[2][0], wa[2][0], WO); DS_TR(w[2][1], wa[2][1], WO); }
            if (i == 5) { DS_TR(w[3][0], wa[3][0], WO); DS_TR(w[3][1], wa[3][1], WO); }
        }
    }
    if (RD) {
#pragma unroll
        for (int t = 0; t < 4; ++t) nxt.W[t] = u32x4{w[t][0][0], w[t][0][1], w[t][1][0], w[t][1][1]};
    }
}

constexpr int G2_GS = 18432;
constexpr int G2_LDS = 65536 + 4 * G2_GS;
constexpr int SLAB = 9216;
constexpr int KB_JUNK = G2_LDS + 512;
constexpr int KB_LDS = KB_JUNK + 256;
struct G2Set { u32x4 W[2]; u32x4 A[9]; };
template <int SLOT, int KS, bool RD>
DEV void g2_half(f32x4 (&acc)[9][2], const G2Set& cur, G2Set& nxt, const u32 (&wa)[2][2], u32 ga0, u32 ga2) {
    constexpr int WO = SLOT * 16384 + KS * 8192, GO = (SLOT & 1) * G2_GS + KS * 9216;
    const u32 ga = SLOT >= 2 ? ga2 : ga0;
    u32x2 w[2][2];
#pragma unroll
    for (int i = 0; i < 18; ++i) {
        MFMA_BF(acc[i / 2][i % 2], cur.A[i / 2], cur.W[i % 2]);
        if (RD) {
            if (i == 0) DS_R128(nxt.A[0], ga, GO);
            if (i == 1) { DS_TR(w[0][0], wa[0][0], WO); DS_TR(w[0][1], wa[0][1], WO); }
            if (i == 2) { DS_TR(w[1][0], wa[1][0], WO); DS_TR(w[1][1], wa[1][1], WO); }
            if (i >= 3 && i <= 10) DS_R128(nxt.A[i - 2], ga, GO + (i - 2) * 256);
        }
    }
    if (RD) {
#pragma unroll
        for (int t = 0; t < 2; ++t) nxt.W[t] = u32x4{w[t][0][0], w[t][0][1], w[t][1][0], w[t][1][1]};
    }
}
#define PFM 11264
#define W2_POL ""
__global__ __launch_bounds__(512, 2) void k_back(const u16* __restrict__ x, const u16* __restrict__ w1, const u16* __restrict__ b1,
                                                 const u16* __restrict__ w2, const u16* __restrict__ b2, u16* __restrict__ out, char* ws STAMP_ARG) {
    extern __shared__ __attribute__((aligned(16))) char smem[];
    const u16* y8 = (const u16*)(ws + WS_Y);
    u16* h = (u16*)(ws + WS_H); u16* hh8 = (u16*)(ws + WS_HH); u16* G8 = (u16*)(ws + WS_G);
    char* slabs = ws + WS_SLAB; u32* ctl = (u32*)(ws + WS_CTL);
    constexpr int ST_FIRST = 32;
    unsigned long long tq37 = 0, tq38 = 0, tq42 = 0; const unsigned long long tqb = __builtin_amdgcn_s_memrealtime();
    const int lane = threadIdx.x & 63;
    const int wave8 = __builtin_amdgcn_readfirstlane(threadIdx.x >> 6);
    const int wave = wave8 & 3;
    STAMP_INIT();
    asm volatile("" :: "s"(x), "s"(w1), "s"(b1), "s"(w2), "s"(b2), "s"(out), "s"(ws));
    const int xg = blockIdx.x & 7, r5 = blockIdx.x >> 3;
    const u32 lbase = lds_addr(smem);
    const int g = lane >> 4, q = (lane & 15) >> 2, p = lane & 3, i16 = lane & 15;
    GAS u32* ctlg = (GAS u32*)ctl;
    const __amdgpu_buffer_rsrc_t rs_h = __builtin_amdgcn_make_buffer_rsrc(h, 0, L_ * D_ * 2, 0x00020000);
    if (wave8 < 4) {
        const int blk = blockIdx.x, rr = lane & 7, j = lane >> 3;
        const int ro = (blk * 8 + rr) * D_ + wave * 144;
        const int uo = (blk * D_ + wave * 144) * 8;
        u32x4 yu[2], xv[2]; u16 y9[2], x9[2];
#pragma unroll
        for (int v = 0; v < 2; ++v) {
            yu[v] = *(const u32x4*)(y8 + uo + 576 * v + lane * 8);
            xv[v] = __builtin_nontemporal_load((const u32x4*)(x + ro + 72 * v + j * 8));
            y9[v] = y8[uo + 576 * v + 512 + lane]; x9[v] = __builtin_nontemporal_load(x + ro + 72 * v + 64 + j);
        }
        char* wb = smem + 131072 + wave * 2048;
        float a[18];
#pragma unroll
        for (int v = 0; v < 2; ++v) {
            *(u32x4*)(wb + v * 1024 + lane * 16) = yu[v];
            const u32x4 yv = tr8x8(wb + v * 1024, lane);
            u32x4 hv;
#pragma unroll
            for (int i = 0; i < 4; ++i) {
                hv[i] = pk_bf16(bf_lo(xv[v][i]) + bf_lo(yv[i]), bf_hi(xv[v][i]) + bf_hi(yv[i]));
                a[9 * v + 2 * i] = bf_lo(hv[i]); a[9 * v + 2 * i + 1] = bf_hi(hv[i]);
            }
            a[9 * v + 8] = rbf(bf2f(x9[v]) + bf2f(y9[v]));
            char* hb = smem + 120832 + (rr * D_ + wave * 144 + 72 * v) * 2;
            *(u32x4*)(hb + j * 16) = hv;
            *(u16*)(hb + 128 + j * 2) = (u16)(__float_as_uint(a[9 * v + 8]) >> 16);
        }
        STAMP0(33);
        float mean;
        const float var = row_stats(a, (f32x2*)(smem + 139264), wave, lane, mean, [&]() { BAR_L0(); });
        const float r1 = __builtin_amdgcn_rsqf(var + 1e-5f);
        const float inv = r1 * __builtin_amdgcn_rsqf(var * r1 * r1 + 1e-5f);
        const float mi = -mean * inv;
        const __amdgpu_buffer_rsrc_t rs_hh = __builtin_amdgcn_make_buffer_rsrc(hh8, 0, L_ * D_ * 2, 0x00020000);
#pragma unroll
        for (int v = 0; v < 2; ++v) {
            float n[9];
#pragma unroll
            for (int i = 0; i < 9; ++i) n[i] = __builtin_fmaf(a[9 * v + i], inv, mi);
            *(u32x4*)(wb + v * 1024 + lane * 16) = u32x4{pk_bf16(n[0], n[1]), pk_bf16(n[2], n[3]), pk_bf16(n[4], n[5]), pk_bf16(n[6], n[7])};
            __builtin_amdgcn_raw_buffer_store_b16((u16)pk_bf16(n[8], n[8]), rs_hh, (uo + 576 * v + 512 + lane) * 2, 0, SC1);
            __builtin_amdgcn_raw_buffer_store_b128(tr8x8(wb + v * 1024, lane), rs_hh, (uo + 576 * v + lane * 8) * 2, 0, SC1);
        }
#pragma unroll
        for (int i = 0; i < 3; ++i) {
            const int o = (i * 256 + (int)threadIdx.x) * 16;
            if (i < 2 || wave == 0) __builtin_amdgcn_raw_buffer_store_b128(*(const u32x4*)(smem + 120832 + o), rs_h, blk * 9216 + o, 0, SC1);
        }
        STAMP0(34);
        WAIT_V(0);
        BAR();
        if (wave8 == 0 && lane < 32) __hip_atomic_fetch_add(ctlg + CTL_E3 + (lane * 8 + (blk >> 5)) * 32, 1u, RLX_AGENT);
        STAMP0(35);
    }
    const int kq = xg >> 1, tile = (xg & 1) * 32 + r5;
    const int s0 = (tile >> 2) * 128, d2 = (tile & 3) * 144;
    const char* w2win = (const char*)w2 + (size_t)kq * (1152 * L_ * 2) + (xg & 1) * 2048;
    const bool g1 = r5 < 27;
    const int id = xg * 27 + r5;
    const int fg = id / 6, mt1 = id % 6;
    const int f0 = fg * 128, d0 = mt1 * 96;
    if (wave8 >= 4) {
        u32 woff[4];
        const char* w2src = (const char*)(w2 + s0) + (size_t)kq * (1152 * L_ * 2) - 4096;
        auto woffs = [&](int pitch) {
#pragma unroll
            for (int i = 0; i < 4; ++i) {
                int row = 16 * wave + 4 * i + (lane >> 4), c = lane & 15;
                int swz = ((row & 3) << 2) | ((row >> 2) & 3);
                woff[i] = row * pitch + ((c ^ swz) << 4) + 4096 - 1024 * i;
            }
        };
        auto issue_w = [&](const char* ws, int slot) { GLDS4(W_POL, lbase + slot * 16384 + 4096 * wave, ws, woff[0], woff[1], woff[2], woff[3]); };
        auto issue_w2 = [&](const char* ws, int slot) { GLDS4(W2_POL, lbase + slot * 16384 + 4096 * wave, ws, woff[0], woff[1], woff[2], woff[3]); };
        if (g1) {
            u32 hoff[3];
            woffs(F_ * 2);
#pragma unroll
            for (int i = 0; i < 3; ++i) {
                int un = 192 * wave + 64 * i + lane;
                hoff[i] = (un / 96) * (D_ * 16) + (un % 96) * 16 + 4096 - 1024 * i;
            }
            const char* wsrc = (const char*)(w1 + f0) - 4096;
            const char* hsrc = (const char*)(hh8 + (size_t)d0 * 8) - 4096;
            auto issue_h = [&](int kt) { GLDS3(" sc1", lbase + 65536 + (kt & 3) * 12288 + 3072 * wave, hsrc + (size_t)kt * (8 * D_ * 16), hoff[0], hoff[1], hoff[2]); };
            auto issue = [&](int kt) { issue_w(wsrc + (size_t)kt * (64 * F_ * 2), kt & 3); issue_h(kt); };
#pragma unroll
            for (int kt = 0; kt < 2; ++kt) issue_w(wsrc + (size_t)kt * (64 * F_ * 2), kt);
            BAR();
            BAR();
#pragma unroll
            for (int kt = 2; kt < 4; ++kt) issue_w(wsrc + (size_t)kt * (64 * F_ * 2), kt);
            BAR();
            issue_h(0); issue_h(1); issue_h(2); issue_h(3);
            WAIT_V(9); BAR();
            WAIT_V(6); BAR(); issue(4);
            WAIT_V(10); BAR(); issue(5);
            for (int kt = 3; kt < 29; ++kt) {
                WAIT_V(14);
                BAR();
                issue(kt + 3);
            }
            woffs(L_ * 2);
            WAIT_V(14); BAR(); issue_w2(w2src, 0);
            WAIT_V(11); BAR(); issue_w2(w2src + 1 * (64 * L_ * 2), 1);
            WAIT_V(8); BAR(); issue_w2(w2src + 2 * (64 * L_ * 2), 2);
            BAR(); issue_w2(w2src + 3 * (64 * L_ * 2), 3);
        } else {
            woffs(L_ * 2);
            const int t = (r5 - 27) * 256 + (threadIdx.x - 256);
            constexpr int P0 = 819 * 9 + PFA * 1280;
            if (PFB > 0) prefetch_lines<9, false>((const char*)w1 + xg * 1152, F_ * 2, P0, 1280, t, 1280);
            BAR();
            if (PFB > 1) prefetch_lines<9, false>((const char*)w1 + xg * 1152, F_ * 2, P0 + 1280, 1280, t, 1280);
            BAR();
            prefetch_lines<9, false>((const char*)w1 + xg * 1152, F_ * 2, P0 + PFB * 1280, 18432 - P0 - PFB * 1280, t, 1280);
            if (wave8 == 4) STAMP(49);
            prefetch_lines<16, false>(w2win, L_ * 2, 0, 4096, t, 1280);
            if (wave8 == 4) STAMP(48);
            prefetch_lines<16, false>(w2win, L_ * 2, 4096 + PFM, 18432 - 4096 - PFM, t, 1280);
            if (wave8 == 4) STAMP(50);
#pragma unroll
            for (int kt = 0; kt < 4; ++kt) issue_w2(w2src + (size_t)kt * (64 * L_ * 2), kt);
        }
        if (wave8 == 4) STAMP(51);
        u32 goff[5];
#pragma unroll
        for (int i = 0; i < 5; ++i) {
            int un = min(64 * i + lane, 287);
            goff[i] = (2 * wave + un / 144) * (D_ * 16) + (un % 144) * 16 + 4096 - 1024 * (i & 3);
        }
        const char* gsrc = (const char*)(G8 + (size_t)d2 * 8) + (size_t)kq * (144 * D_ * 16) - 4096;
        auto issue_g = [&](int kt) {
            const char* gs = gsrc + (size_t)kt * (8 * D_ * 16);
            const u32 gd = lbase + 65536 + (kt & 3) * G2_GS + 4608 * wave;
            GLDS4(" sc1", gd, gs, goff[0], goff[1], goff[2], goff[3]);
            if (lane < 32) GLDS1(" sc1", gd + 4096, gs, goff[4]);
        };
        auto issue2 = [&](int kt) { issue_w2(w2src + (size_t)kt * (64 * L_ * 2), kt & 3); issue_g(kt); };
        if (wave8 == 4) {
            const unsigned long long t0 = __builtin_amdgcn_s_memrealtime();
            GAS u32* c = ctlg + CTL_E4 + (((kq * 4 + (tile & 3)) * 4 + ((tile >> 2) & 3)) * 4 + (lane & 3)) * 32;
            for (;;) {
                int v = (int)__hip_atomic_load(c, RLX_AGENT);
                v += __builtin_amdgcn_update_dpp(0, v, 0xB1, 0xf, 0xf, true);
                v += __builtin_amdgcn_update_dpp(0, v, 0x4E, 0xf, 0xf, true);
                if (__builtin_amdgcn_readfirstlane(v) >= 54) break;
                if (__builtin_amdgcn_s_memrealtime() - t0 > SPIN_LIMIT) break;
                __builtin_amdgcn_s_sleep(2);
            }
        }
        asm volatile("" ::: "memory");
        BAR();
        issue_g(0); issue_g(1); issue_g(2); issue_g(3);
        WAIT_V(15); BAR();
        WAIT_V(10); BAR(); issue2(4);
        WAIT_V(14); BAR(); issue2(5);
        for (int kt = 3; kt < 15; ++kt) {
            WAIT_V(18);
            BAR();
            issue2(kt + 3);
        }
        WAIT_V(18); BAR();
        WAIT_V(9); BAR();
        WAIT_V(0); BAR();
    } else if (g1) {
        const int wf = wave >> 1, wd = wave & 1;
        u32 wa[4][2];
#pragma unroll
        for (int t = 0; t < 4; ++t)
#pragma unroll
            for (int tau = 0; tau < 2; ++tau) {
                int row = 8 * g + 4 * tau + q;
                int swz = (q << 2) | ((2 * g + tau) & 3);
                int ch = 2 * (4 * wf + t) + (p >> 1);
                wa[t][tau] = lbase + row * 256 + ((ch ^ swz) << 4) + 8 * (p & 1);
            }
        const u32 ha = lbase + 65536 + g * 1536 + (48 * wd + i16) * 16;
        f32x4 acc[4][3];
#pragma unroll
        for (int t = 0; t < 4; ++t) {
            const u32x2 bw = *(const u32x2*)(b1 + f0 + 64 * wf + 16 * t + 4 * g);
#pragma unroll
            for (int m = 0; m < 3; ++m) { acc[t][m] = f32x4{bf_lo(bw[0]), bf_hi(bw[0]), bf_lo(bw[1]), bf_hi(bw[1])}; PIN(acc[t][m]); }
        }
        G1Set X, Y;
#pragma unroll
        for (int t = 0; t < 4; ++t) { Y.W[t] = u32x4{0, 0, 0, 0}; asm volatile("" : "+v"(Y.W[t])); }
#pragma unroll
        for (int m = 0; m < 3; ++m) { Y.H[m] = u32x4{0, 0, 0, 0}; asm volatile("" : "+v"(Y.H[m])); }
        if (wave8 == 0) {
            const unsigned long long t0 = __builtin_amdgcn_s_memrealtime();
            GAS u32* c = ctlg + CTL_E3 + ((blockIdx.x & 31) * 8 + (lane & 7)) * 32;
            for (;;) {
                u32 v = __hip_atomic_load(c, RLX_AGENT);
                if (__all(v >= 32u)) break;
                if (__builtin_amdgcn_s_memrealtime() - t0 > SPIN_LIMIT) break;
                __builtin_amdgcn_s_sleep(1);
            }
        }
        STAMP0(36);
        asm volatile("" ::: "memory");
        BAR();
        asm volatile("s_nop 1");
        for (int kt = 0; kt < 32; kt += 4) {
            BAR_L0();
            if (kt == 0) { STAMP0(37); CSTAMP0(60); }
            g1_half<0, 0, true>(acc, Y, X, wa, ha); WAIT_L0();
            g1_half<0, 1, true>(acc, X, Y, wa, ha);
            BAR_L0();
            g1_half<1, 0, true>(acc, Y, X, wa, ha); WAIT_L0();
            g1_half<1, 1, true>(acc, X, Y, wa, ha);
            BAR_L0();
            g1_half<2, 0, true>(acc, Y, X, wa, ha); WAIT_L0();
            g1_half<2, 1, true>(acc, X, Y, wa, ha);
            BAR_L0();
            g1_half<3, 0, true>(acc, Y, X, wa, ha); WAIT_L0();
            g1_half<3, 1, true>(acc, X, Y, wa, ha);
        }
        BAR_L0();
        g1_half<0, 0, false>(acc, Y, X, wa, ha);
        FENCE6(acc[2][0], acc[2][1], acc[2][2], acc[3][0], acc[3][1], acc[3][2]);
#pragma unroll
        for (int t = 0; t < 2; ++t)
#pragma unroll
            for (int m = 0; m < 3; ++m) PIN(acc[t][m]);
        STAMP0(38); CSTAMP0(61);
        const __amdgpu_buffer_rsrc_t rs_g = __builtin_amdgcn_make_buffer_rsrc(G8, 0, D_ * F_ * 2, 0x00020000);
        const int go = (((f0 + 64 * wf) / 8 + 2 * (g & 1) + (g >> 1)) * D_ + d0 + 48 * wd + i16) * 16;
#pragma unroll
        for (int t = 0; t < 4; t += 2) {
#pragma unroll
            for (int m = 0; m < 3; ++m) {
                u32 o[2][2];
#pragma unroll
                for (int k = 0; k < 2; ++k) {
                    const f32x4 v = acc[t + k][m];
                    const f32x2 lo = gelu2(f32x2{v[0], v[1]}), hi = gelu2(f32x2{v[2], v[3]});
                    o[k][0] = pk_bf16(lo[0], lo[1]);
                    o[k][1] = pk_bf16(hi[0], hi[1]);
                }
                auto r0 = __builtin_amdgcn_permlane16_swap(o[0][0], o[1][0], false, false);
                auto r1 = __builtin_amdgcn_permlane16_swap(o[0][1], o[1][1], false, false);
                __builtin_amdgcn_raw_buffer_store_b128(u32x4{r0[0], r1[0], r0[1], r1[1]}, rs_g, go + (2 * t * D_ + 16 * m) * 16, 0, SC1);
            }
        }
        STAMP0(39);
        WAIT_V(0);
        if (lane < 4) __hip_atomic_fetch_add(ctlg + CTL_E4 + ((((fg / 9) * 4 + (2 * mt1 + wd) / 3) * 4 + lane) * 4 + (fg & 3)) * 32, 1u, RLX_AGENT);
        STAMP0(40);
    }
    const __amdgpu_buffer_rsrc_t rs_s = __builtin_amdgcn_make_buffer_rsrc(slabs + (size_t)tile * (16 * SLAB), 0, 16 * SLAB, 0x00020000);
    GAS u32* fl = ctlg + CTL_XF + tile * 128;
    const int s = s0 + 32 * kq + i16;
    u32x2 hv[2][2];
    float bb[2];
    auto load_res = [&]() {
#pragma unroll
        for (int j = 0; j < 2; ++j)
#pragma unroll
            for (int t = 0; t < 2; ++t)
                hv[j][t] = __builtin_amdgcn_raw_buffer_load_b64(rs_h, ((s + 16 * t) * D_ + d2 + 16 * (j ? 8 : wave8) + 4 * g) * 2, 0, SC1);
        bb[0] = bf2f(b2[s]); bb[1] = bf2f(b2[s + 16]);
    };
    if (wave8 < 4) {
        if (g1) {
            constexpr int PW = (PFM + 107) / 108;
            prefetch_pair<16>(lbase + KB_JUNK, w2win, L_ * 2, 4096 + min((r5 * 4 + wave) * PW + lane, PFM - 1));
            if (lane < PW - 64) prefetch_pair<16>(lbase + KB_JUNK, w2win, L_ * 2, 4096 + min((r5 * 4 + wave) * PW + 64 + lane, PFM - 1));
        }
        u32 wa[2][2];
#pragma unroll
        for (int t = 0; t < 2; ++t)
#pragma unroll
            for (int tau = 0; tau < 2; ++tau) {
                int row = 8 * g + 4 * tau + q;
                int swz = (q << 2) | ((2 * g + tau) & 3);
                int ch = 2 * (2 * wave + t) + (p >> 1);
                wa[t][tau] = lbase + row * 256 + ((ch ^ swz) << 4) + 8 * (p & 1);
            }
        const u32 ga0 = lbase + 65536 + g * 2304 + i16 * 16, ga2 = ga0 + 2 * G2_GS;
        f32x4 acc[9][2];
#pragma unroll
        for (int m = 0; m < 9; ++m)
#pragma unroll
            for (int t = 0; t < 2; ++t) { acc[m][t] = f32x4{0, 0, 0, 0}; asm volatile("" : "+v"(acc[m][t])); }
        G2Set X, Y;
#pragma unroll
        for (int t = 0; t < 2; ++t) { Y.W[t] = u32x4{0, 0, 0, 0}; asm volatile("" : "+v"(Y.W[t])); }
#pragma unroll
        for (int m = 0; m < 9; ++m) { Y.A[m] = u32x4{0, 0, 0, 0}; asm volatile("" : "+v"(Y.A[m])); }
        asm volatile("" ::: "memory");
        BAR();
        asm volatile("" ::: "memory");
        STAMP0(41);
        load_res();
        asm volatile("s_nop 1");
        for (int kt = 0; kt < 16; kt += 4) {
            BAR_L0();
            if (kt == 0) { STAMP0(42); CSTAMP0(62); }
            g2_half<0, 0, true>(acc, Y, X, wa, ga0, ga2); WAIT_L0();
            g2_half<0, 1, true>(acc, X, Y, wa, ga0, ga2);
            BAR_L0();
            g2_half<1, 0, true>(acc, Y, X, wa, ga0, ga2); WAIT_L0();
            g2_half<1, 1, true>(acc, X, Y, wa, ga0, ga2);
            BAR_L0();
            g2_half<2, 0, true>(acc, Y, X, wa, ga0, ga2); WAIT_L0();
            g2_half<2, 1, true>(acc, X, Y, wa, ga0, ga2);
            BAR_L0();
            g2_half<3, 0, true>(acc, Y, X, wa, ga0, ga2); WAIT_L0();
            g2_half<3, 1, true>(acc, X, Y, wa, ga0, ga2);
        }
        BAR_L0();
        g2_half<0, 0, true>(acc, Y, X, wa, ga0, ga2); WAIT_L0();
        g2_half<0, 1, true>(acc, X, Y, wa, ga0, ga2);
        BAR_L0();
        g2_half<1, 0, true>(acc, Y, X, wa, ga0, ga2); WAIT_L0();
        g2_half<1, 1, true>(acc, X, Y, wa, ga0, ga2);
        WAIT_L0();
        g2_half<0, 0, false>(acc, Y, X, wa, ga0, ga2);
        FENCE6(acc[6][0], acc[6][1], acc[7][0], acc[7][1], acc[8][0], acc[8][1]);
#pragma unroll
        for (int m = 0; m < 6; ++m)
#pragma unroll
            for (int t = 0; t < 2; ++t) PIN(acc[m][t]);
        STAMP0(43); CSTAMP0(63);
        if (wave == kq) {
            f32x4* own = (f32x4*)(smem + 32768);
#pragma unroll
            for (int i = 0; i < 18; ++i) own[i * 64 + lane] = acc[i >> 1][i & 1];
        } else {
            const int so = (wave * 4 + kq) * SLAB + lane * 16;
#pragma unroll
            for (int m = 0; m < 9; ++m)
                __builtin_amdgcn_raw_buffer_store_b128(u32x4{pk_bf16(acc[m][0][0], acc[m][0][1]), pk_bf16(acc[m][0][2], acc[m][0][3]),
                                                             pk_bf16(acc[m][1][0], acc[m][1][1]), pk_bf16(acc[m][1][2], acc[m][1][3])}, rs_s, so + m * 1024, 0, SC1);
            WAIT_V(0);
            if (lane == 0) __hip_atomic_store(fl + wave * 32 + kq, 1u, RLX_AGENT);
        }
        if (wave == ((kq + 1) & 3)) STAMP(44);
    } else {
        load_res();
        if (wave8 == 4) {
            const unsigned long long t0 = __builtin_amdgcn_s_memrealtime();
            for (;;) {
                u32 f = __hip_atomic_load(fl + kq * 32 + (lane & 3), RLX_AGENT);
                if (__all(f == 1u || (lane & 3) == kq)) break;
                if (__builtin_amdgcn_s_memrealtime() - t0 > SPIN_LIMIT) break;
                __builtin_amdgcn_s_sleep(2);
            }
        }
    }
    BAR_L0();
    STAMP0(45);
    u32x4 part[2][3];
#pragma unroll
    for (int j = 0; j < 2; ++j) {
        const int po = j == 0 ? wave8 * 1024 + lane * 16 : (wave8 == 0 ? 8192 + lane * 16 : 0x40000000);
#pragma unroll
        for (int k = 0; k < 3; ++k) part[j][k] = __builtin_amdgcn_raw_buffer_load_b128(rs_s, (kq * 4 + ((kq + 1 + k) & 3)) * SLAB + po, 0, SC1);
    }
#pragma unroll
    for (int j = 0; j < 2; ++j) {
        if (j == 1 && wave8 != 0) break;
        const int m = j ? 8 : wave8;
#pragma unroll
        for (int t = 0; t < 2; ++t) {
            f32x4 v = ((const f32x4*)(smem + 32768))[(m * 2 + t) * 64 + lane];
#pragma unroll
            for (int k = 0; k < 3; ++k) v += f32x4{bf_lo(part[j][k][2 * t]), bf_hi(part[j][k][2 * t]), bf_lo(part[j][k][2 * t + 1]), bf_hi(part[j][k][2 * t + 1])};
            const u32 y0 = pk_bf16(v[0] + bb[t], v[1] + bb[t]), y1 = pk_bf16(v[2] + bb[t], v[3] + bb[t]);
            *(u32x2*)(out + (size_t)(s + 16 * t) * D_ + d2 + 16 * m + 4 * g) =
                u32x2{pk_bf16(bf_lo(hv[j][t][0]) + bf_lo(y0), bf_hi(hv[j][t][0]) + bf_hi(y0)), pk_bf16(bf_lo(hv[j][t][1]) + bf_lo(y1), bf_hi(hv[j][t][1]) + bf_hi(y1))};
        }
    }
    if (blockIdx.x == 8 && wave8 == 0) {
        WAIT_V(0);
        const unsigned long long te = __builtin_amdgcn_s_memrealtime();
        const unsigned long long* dg = (const unsigned long long*)(ws + (100u << 20));
        const unsigned long long a0 = dg[0], a1 = dg[1];
        unsigned long long iv = DIAG == 1 ? a1 - a0 : DIAG == 2 ? tqb - a1 : DIAG == 3 ? tq37 - tqb : DIAG == 4 ? tq38 - tq37 : DIAG == 5 ? tq42 - tq38 : DIAG == 6 ? te - tq42 : te - a0;
        if (iv > 10000) iv = 10000;
        while (__builtin_amdgcn_s_memrealtime() - te < 4 * iv + 500) __builtin_amdgcn_s_sleep(1);
    }
    asm volatile(".Lcode_end:");
}

extern "C" void kernel_launch(void* const* d_in, const int* in_sizes, int n_in,
                              void* d_out, int out_size, void* d_ws, size_t ws_size,
                              hipStream_t stream) {
    const u16* x = (const u16*)d_in[0];
    const u16* logA = (const u16*)d_in[1];
    const u16* Aim = (const u16*)d_in[2];
    const u16* Cre = (const u16*)d_in[3];
    const u16* Cim = (const u16*)d_in[4];
    const u16* logdt = (const u16*)d_in[5];
    const u16* Dskip = (const u16*)d_in[6];
    const u16* w1 = (const u16*)d_in[8];
    const u16* b1 = (const u16*)d_in[9];
    const u16* w2 = (const u16*)d_in[10];
    const u16* b2 = (const u16*)d_in[11];
    char* ws = (char*)d_ws;
    (void)hipFuncSetAttribute((const void*)k_front, hipFuncAttributeMaxDynamicSharedMemorySize, LDS_REQ(KA_LDS));
    (void)hipFuncSetAttribute((const void*)k_back, hipFuncAttributeMaxDynamicSharedMemorySize, LDS_REQ(KB_LDS));
    k_front<<<256, 512, LDS_REQ(KA_LDS), stream>>>(x, ws, logA, Aim, Cre, Cim, logdt, Dskip, w1 STAMP_PASS);
    k_back<<<256, 512, LDS_REQ(KB_LDS), stream>>>(x, w1, b1, w2, b2, (u16*)d_out, ws STAMP_PASS);
}
```

```cpp
#define NOPFM
#include <hip/hip_runtime.h>
#include <stdint.h>

typedef unsigned short u16;
typedef unsigned int u32;
typedef short s16x4 __attribute__((ext_vector_type(4)));
typedef short s16x8 __attribute__((ext_vector_type(8)));
typedef __bf16 bf16x8 __attribute__((ext_vector_type(8)));
typedef __bf16 bf16x2 __attribute__((ext_vector_type(2)));
typedef _Float16 f16x8 __attribute__((ext_vector_type(8)));
typedef _Float16 f16x2 __attribute__((ext_vector_type(2)));
typedef float f32x4 __attribute__((ext_vector_type(4)));
typedef float f32x2 __attribute__((ext_vector_type(2)));
typedef u32 u32x4 __attribute__((ext_vector_type(4)));
typedef u32 u32x2 __attribute__((ext_vector_type(2)));
#define LAS __attribute__((address_space(3)))
#define DEV __device__ __forceinline__

constexpr int L_ = 2048, D_ = 576, N_ = 128, F_ = 4608;

#define STAMP(id)
#define CSTAMP(id)
#define STAMP0(id)
#define CSTAMP0(id)
#define STAMP_INIT()
#define STAMP_DUMP()
#define STAMP_ARG
#define STAMP_PASS
#define LDS_REQ(n) (n)

DEV float bf_lo(u32 w) { return __uint_as_float(w << 16); }
DEV float bf_hi(u32 w) { return __uint_as_float(w & 0xffff0000u); }
DEV float bf2f(u16 v) { return __uint_as_float(((u32)v) << 16); }
DEV u32 pk_bf16(float a, float b) { f32x2 v = {a, b}; return __builtin_bit_cast(u32, __builtin_convertvector(v, bf16x2)); }
DEV u32 pk_f16(float a, float b) { f32x2 v = {a, b}; return __builtin_bit_cast(u32, __builtin_convertvector(v, f16x2)); }
DEV float rbf(float a) { return bf_lo(pk_bf16(a, a)); }
DEV float h_lo(u32 w) { return (float)__builtin_bit_cast(f16x2, w)[0]; }
DEV float h_hi(u32 w) { return (float)__builtin_bit_cast(f16x2, w)[1]; }

DEV u32 lds_addr(const void* p) { return (u32)(uintptr_t)(LAS const char*)p; }
template <int CTRL> DEV float dpp_get(float v) { return __builtin_bit_cast(float, __builtin_amdgcn_update_dpp(0, __builtin_bit_cast(int, v), CTRL, 0xf, 0xf, true)); }
DEV float swap32_add(float v) {
    auto r = __builtin_amdgcn_permlane32_swap(__float_as_uint(v), __float_as_uint(v), false, false);
    return __uint_as_float(r[0]) + __uint_as_float(r[1]);
}
DEV float swap16_add(float v) {
    auto r = __builtin_amdgcn_permlane16_swap(__float_as_uint(v), __float_as_uint(v), false, false);
    return __uint_as_float(r[0]) + __uint_as_float(r[1]);
}
DEV float sum_j(float v) { return swap32_add(swap16_add(v + dpp_get<0x128>(v))); }
DEV float sum_j4(float v) { return swap16_add(v + dpp_get<0x128>(v)); }
DEV u32 umax(u32 a, u32 b) { return a > b ? a : b; }
template <int CTRL> DEV u32 dpp_umax(u32 v) { return umax(v, (u32)__builtin_amdgcn_update_dpp(0, (int)v, CTRL, 0xf, 0xf, true)); }
DEV u32 wave_umax(u32 v) {
    v = dpp_umax<0xB1>(v); v = dpp_umax<0x4E>(v); v = dpp_umax<0x141>(v); v = dpp_umax<0x140>(v);
    { auto r = __builtin_amdgcn_permlane16_swap(v, v, false, false); v = umax(r[0], r[1]); }
    { auto r = __builtin_amdgcn_permlane32_swap(v, v, false, false); v = umax(r[0], r[1]); }
    return v;
}
DEV u32x4 tr8x8(const char* wbuf, int lane) {
    const int q = (lane & 15) >> 2, p = lane & 3;
    const char* a = wbuf + ((lane >> 4) * 16 + (p >> 1) * 8 + q) * 16 + (p & 1) * 8;
    s16x4 lo = __builtin_amdgcn_ds_read_tr16_b64_v4i16((LAS s16x4*)a);
    s16x4 hi = __builtin_amdgcn_ds_read_tr16_b64_v4i16((LAS s16x4*)(a + 64));
    return __builtin_bit_cast(u32x4, __builtin_shufflevector(lo, hi, 0, 1, 2, 3, 4, 5, 6, 7));
}

#define MFMA_BF(acc, a, b) asm volatile("v_mfma_f32_16x16x32_bf16 %0, %1, %2, %0" : "+v"(acc) : "v"(a), "v"(b))
#define MFMA_F16(acc, a, b) asm volatile("v_mfma_f32_16x16x32_f16 %0, %1, %2, %0" : "+v"(acc) : "v"(a), "v"(b))
#define DS_R128(dst, addr, off) asm volatile("ds_read_b128 %0, %1 offset:%2" : "=v"(dst) : "v"(addr), "i"(off))
#define DS_TR(dst, addr, off) asm volatile("ds_read_b64_tr_b16 %0, %1 offset:%2" : "=v"(dst) : "v"(addr), "i"(off))
#define WAIT_L0() asm volatile("s_waitcnt lgkmcnt(0)" ::: "memory")
#define WAIT_V(n) asm volatile("s_waitcnt vmcnt(" #n ")" ::: "memory")
#define BAR_L0() asm volatile("s_waitcnt lgkmcnt(0)\n\ts_barrier" ::: "memory")
#define BAR() asm volatile("s_barrier" ::: "memory")
#define PIN(x) asm volatile("" : "+v"(x))
#define FENCE6(a0, a1, a2, a3, a4, a5) asm volatile("s_nop 7\n\ts_nop 3" : "+v"(a0), "+v"(a1), "+v"(a2), "+v"(a3), "+v"(a4), "+v"(a5) :: "memory")
#define FENCE8(a) asm volatile("s_nop 7\n\ts_nop 3" : "+v"(a[0]), "+v"(a[1]), "+v"(a[2]), "+v"(a[3]), "+v"(a[4]), "+v"(a[5]), "+v"(a[6]), "+v"(a[7]) :: "memory")
#define GLDS4(POL, lds, base, o0, o1, o2, o3) asm volatile("s_mov_b32 m0, %0\n\ts_nop 0\n\t" \
    "global_load_lds_dwordx4 %2, %1" POL "\n\tglobal_load_lds_dwordx4 %3, %1 offset:1024" POL "\n\t" \
    "global_load_lds_dwordx4 %4, %1 offset:2048" POL "\n\tglobal_load_lds_dwordx4 %5, %1 offset:3072" POL \
    :: "s"(lds), "s"(base), "v"(o0), "v"(o1), "v"(o2), "v"(o3) : "memory")
#define GLDS3(POL, lds, base, o0, o1, o2) asm volatile("s_mov_b32 m0, %0\n\ts_nop 0\n\t" \
    "global_load_lds_dwordx4 %2, %1" POL "\n\tglobal_load_lds_dwordx4 %3, %1 offset:1024" POL "\n\t" \
    "global_load_lds_dwordx4 %4, %1 offset:2048" POL \
    :: "s"(lds), "s"(base), "v"(o0), "v"(o1), "v"(o2) : "memory")
#define GLDS1(POL, lds, base, o0) asm volatile("s_mov_b32 m0, %0\n\ts_nop 0\n\t" \
    "global_load_lds_dwordx4 %2, %1" POL :: "s"(lds), "s"(base), "v"(o0) : "memory")

#define GAS __attribute__((address_space(1)))
#define RLX_AGENT __ATOMIC_RELAXED, __HIP_MEMORY_SCOPE_AGENT
constexpr int SC1 = 16;
constexpr unsigned long long SPIN_LIMIT = 2000000ull;
constexpr int CTL_E3 = 0, CTL_E4 = 8192, CTL_XF = 16384;

constexpr size_t SZ = (size_t)L_ * D_ * 2;
constexpr size_t WS_CTL = 0, WS_EP = 98304, WS_U = 131072, WS_Y = WS_U + SZ, WS_HH = WS_Y + SZ, WS_H = WS_HH + SZ, WS_G = WS_H + SZ,
                 WS_SLAB = WS_G + (size_t)D_ * F_ * 2, WS_END = WS_SLAB + (size_t)64 * 16 * 9216;
static_assert(WS_END < (64u << 19), "the translation warm-up covers 64 x 512 KB");

template <int LPR, bool NT>
DEV void prefetch_lines(const char* base, u32 pitch, int first, int count, int t, int stride) {
    u32 prev = 0;
    for (int ln = t; ln < count; ln += stride) {
        const u32 l = first + ln;
        const u32* a = (const u32*)(base + (size_t)(l / LPR) * pitch + (l % LPR) * 128);
        const u32 v = NT ? __builtin_nontemporal_load(a) : *a;
        asm volatile("" :: "v"(prev));
        prev = v;
    }
    asm volatile("" :: "v"(prev));
}

template <int LPR>
DEV void prefetch_pair(u32 lds_junk, const char* base, u32 pitch, u32 l) {
    const u32 off = (l / LPR) * pitch + (l % LPR) * 128;
    asm volatile("s_mov_b32 m0, %0\n\ts_nop 0\n\tglobal_load_lds_dword %1, %2 nt\n\tglobal_load_lds_dword %1, %2" :: "s"(lds_junk), "v"(off), "s"(base) : "memory");
}
DEV void lds_arrive(volatile LAS u32* c, int lane) { WAIT_L0(); if (lane == 0) __hip_atomic_fetch_add((LAS u32*)c, 1u, __ATOMIC_RELAXED, __HIP_MEMORY_SCOPE_WORKGROUP); }
template <int SLEEP> DEV void lds_wait(volatile LAS u32* c, u32 n) { while (*c < n) { if (SLEEP) __builtin_amdgcn_s_sleep(SLEEP); } asm volatile("" ::: "memory"); }

template <class SYNC>
DEV float row_stats(const float (&a)[18], f32x2* red, int wave, int lane, float& mean_out, SYNC sync) {
    float s0 = a[0] + a[1], s1 = a[2] + a[3];
#pragma unroll
    for (int i = 4; i < 18; i += 2) { s0 += a[i]; s1 += a[i + 1]; }
    const float mw = sum_j(s0 + s1) * (1.0f / 144);
    float q0 = 0, q1 = 0;
#pragma unroll
    for (int i = 0; i < 18; i += 2) { const float d0 = a[i] - mw, d1 = a[i + 1] - mw; q0 = __builtin_fmaf(d0, d0, q0); q1 = __builtin_fmaf(d1, d1, q1); }
    const float q = sum_j(q0 + q1);
    if (lane < 8) red[wave * 8 + lane] = f32x2{mw, q};
    sync();
    const f32x2 pj = red[lane & 31];
    const float mean = sum_j4(pj[0]) * 0.25f;
    const float dj = pj[0] - mean;
    const float m2 = sum_j4(__builtin_fmaf(144.0f * dj, dj, pj[1]));
    mean_out = mean;
    return m2 * (1.0f / D_);
}

typedef f32x2 cf;
DEV cf cmul(cf p, cf q) {
    cf t, r;
    asm("v_pk_mul_f32 %0, %1, %2 op_sel:[0,0] op_sel_hi:[0,1]" : "=v"(t) : "v"(p), "v"(q));
    asm("v_pk_fma_f32 %0, %1, %2, %3 op_sel:[1,1,0] op_sel_hi:[1,0,1] neg_lo:[1,0,0]" : "=v"(r) : "v"(p), "v"(q), "v"(t));
    return r;
}
DEV u32 hmul(u32 p, u32 q) {
    u32 t, r;
    asm("v_pk_mul_f16 %0, %1, %2 op_sel:[0,0] op_sel_hi:[0,1]" : "=v"(t) : "v"(p), "v"(q));
    asm("v_pk_fma_f16 %0, %1, %2, %3 op_sel:[1,1,0] op_sel_hi:[1,0,1] neg_lo:[1,0,0]" : "=v"(r) : "v"(p), "v"(q), "v"(t));
    return r;
}
DEV u32 toh(cf v) { return pk_f16(v[0], v[1]); }
DEV cf cexp_s(float zr2, float zi, float s) {
    const float mag = __builtin_amdgcn_exp2f(zr2 * s);
    const float th = zi * s;
    const float c_hi = 0.15915494309189535f, c_lo = 6.4206382e-9f;
    const float hi = th * c_hi;
    const float lo = __builtin_fmaf(th, c_hi, -hi) + th * c_lo;
    const float rev = __builtin_amdgcn_fractf(hi) + lo;
    return cf{mag * __builtin_amdgcn_cosf(rev), mag * __builtin_amdgcn_sinf(rev)};
}
constexpr int TKS = 57 * 64;
constexpr int TBL = 8 * TKS;
constexpr int KA_K = 3 * TBL;
constexpr int KCH = 4160;
constexpr int KA_U = KA_K + 3 * KCH;
constexpr int KA_WB = KA_U + 288 * 48;
constexpr int KA_MISC = KA_WB + 8192;
constexpr int KA_JUNK = KA_MISC + 512;
constexpr int KA_LDS = KA_JUNK + 256;
constexpr int SCH = 2 * 3088 + 2 * 2048;
constexpr int KA_RED = 3 * SCH, KA_OUT = KA_RED + 24576;
static_assert(KA_OUT + 12288 <= KA_K && KA_K % 16 == 0 && KA_RED % 16 == 0, "LDS map");

DEV u32 pk_add_h(u32 a, u32 b) { return __builtin_bit_cast(u32, __builtin_bit_cast(f16x2, a) + __builtin_bit_cast(f16x2, b)); }
DEV u32x4 ld4(const char* p) { const u32* w = (const u32*)p; return u32x4{w[0], w[1], w[2], w[3]}; }
template <int CP>
DEV void put4(u32 p, float v0, float v1, float v2, float v3) {
    const u32 p32 = pk_f16(v3, v2), p10 = pk_f16(v1, v0), p21 = pk_f16(v2, v1);
    asm volatile("ds_write_b64 %0, %1 offset:%4\n\tds_write_b16 %0, %5 offset:2\n\tds_write_b32 %0, %2 offset:4\n\tds_write_b16_d16_hi %0, %3 offset:8"
                 :: "v"(p), "v"(u32x2{p32, p10}), "v"(p21), "v"(p10), "i"(CP + 4), "v"(p32) : "memory");
}
struct B8 { u32x4 f[8]; };
#define MFMA_F16_Z(acc, a, b) asm volatile("v_mfma_f32_16x16x32_f16 %0, %1, %2, 0" : "=&v"(acc) : "v"(a), "v"(b))
#define DS_W128(addr, src, off) asm volatile("ds_write_b128 %0, %1 offset:%2" :: "v"(addr), "v"(src), "i"(off) : "memory")
template <int MODE, bool ZERO = false, int FL = -1>
DEV void cv_dense(f32x4 (&acc)[8], const u32x4& A0, const u32x4& A1, const B8& cur, B8& nxt, u32 ba, const f32x4* old = nullptr, u32 ra = 0) {
    u32x2 h[8][2];
#pragma unroll
    for (int k = 0; k < 15; ++k) {
        if (k < 8) { if (ZERO) MFMA_F16_Z(acc[k], A0, cur.f[k]); else MFMA_F16(acc[k], A0, cur.f[k]); }
        else MFMA_F16(acc[k - 7], A1, cur.f[k - 8]);
        if (MODE == 1 && k < 8) DS_R128(nxt.f[k], ba, 1536 * k);
        if (MODE == 2 && k < 8) { DS_TR(h[k][0], ba, 512 * (k >> 1) + 16 * (1 - (k & 1))); DS_TR(h[k][1], ba, 512 * (k >> 1) + 128 + 16 * (1 - (k & 1))); }
        if (FL >= 0 && k >= 7) DS_W128(ra, old[k - 7], (FL * 8 + k - 7) * 1024);
    }
    if (MODE == 2) {
#pragma unroll
        for (int k = 0; k < 8; ++k) nxt.f[k] = u32x4{h[k][0][0], h[k][0][1], h[k][1][0], h[k][1][1]};
    }
}
struct KParam { u32 la, ai, cr, ci; u16 dt, ds; };
DEV KParam kparam_load(const u16* logA, const u16* Aim, const u16* Cre, const u16* Cim, const u16* logdt, const u16* Dskip, int d, int lane) {
    const int o = d * N_ + 2 * lane;
    return KParam{__builtin_nontemporal_load((const u32*)(logA + o)), __builtin_nontemporal_load((const u32*)(Aim + o)),
                  __builtin_nontemporal_load((const u32*)(Cre + o)), __builtin_nontemporal_load((const u32*)(Cim + o)),
                  __builtin_nontemporal_load(logdt + d), __builtin_nontemporal_load(Dskip + d)};
}
DEV void table_task(const KParam& k, int c, int hf, char* smem, f32x2* chp, int lane) {
    const int sh = hf ? 0 : 16, i16 = lane & 15;
    const float dt = rbf(__expf(bf2f(k.dt))), dsk = bf2f(k.ds);
    const float zr = -(rbf(__expf(bf_hi(k.la << sh))) * dt) * 1.4426950408889634f, zi = bf_hi(k.ai << sh) * dt;
    const float cs = __uint_as_float(wave_umax(umax(umax(k.cr & 0x7fff0000u, (k.cr << 16) & 0x7fff0000u), umax(k.ci & 0x7fff0000u, (k.ci << 16) & 0x7fff0000u))));
    const float ksc = fmaxf(fmaxf(2.0f * dt * cs, fabsf(dsk) * (1.0f / 1024)), 1e-30f);
    const float rs = __builtin_amdgcn_rcpf(ksc);
    const cf C = cf{bf_hi(k.cr << sh), bf_hi(k.ci << sh)} * (2.0f * dt * rs);
    char* tb = smem + c * TBL + (hf * 4 + (lane >> 4)) * TKS;
    auto put = [&](int row, u32 v) { *(u32*)(tb + row * 64 + ((i16 ^ (((-(row >> 2)) & 3) << 2)) << 2)) = v; };
    const cf e1 = cexp_s(zr, zi, 1.f), w1 = cexp_s(zr, -zi, 16.f), g1 = cexp_s(zr, zi, 256.f);
    const cf e2 = cmul(e1, e1), e3 = cmul(e2, e1), e4 = cmul(e2, e2);
    const cf w2 = cmul(w1, w1), w3 = cmul(w2, w1), w4 = cmul(w2, w2), w8 = cmul(w4, w4), w12 = cmul(w8, w4);
    const u32 f[4] = {0, toh(e1), toh(e2), toh(e3)};
    const u32 wf[4] = {0x00003c00u, toh(w1), toh(w2), toh(w3)}, wq[4] = {0, toh(w4), toh(w8), toh(w12)};
    u32 cq[5], gk[8];
    { cf t = C; cq[0] = toh(t);
#pragma unroll
      for (int a = 1; a < 5; ++a) { t = cmul(t, e4); cq[a] = toh(t); } }
    { cf t = g1; gk[1] = toh(t);
#pragma unroll
      for (int a = 2; a < 8; ++a) { t = cmul(t, g1); gk[a] = toh(t); } }
#pragma unroll
    for (int a = 0; a < 4; ++a)
#pragma unroll
        for (int b = 0; b < 4; ++b) {
            put(4 * a + b, a == 0 ? wf[b] : b == 0 ? wq[a] : hmul(wq[a], wf[b]));
            const u32 r0 = b == 0 ? cq[a] : hmul(cq[a], f[b]);
            put(16 + 4 * a + b, r0);
            if ((b & 1) == 0) put(48 + 2 * a + (b >> 1), hmul(r0, gk[1]));
        }
    put(56, hmul(cq[4], gk[1]));
#pragma unroll
    for (int i = 0; i < 4; ++i) {
        put(32 + i, hmul(cq[i + 1], gk[2])); put(36 + i, hmul(cq[i + 1], gk[3]));
        put(40 + 2 * i, hmul(cq[2], gk[4 + i])); put(41 + 2 * i, hmul(cq[4], gk[4 + i]));
    }
    if (lane == 0 && hf == 0) chp[c] = f32x2{ksc, dsk * rs};
}

#define POLL1_FIRST 26
#define POLL1_SLEEP 2
#define PFA 0
#define PFB 0
#define PFA_DELAY 330
__global__ __launch_bounds__(512, 2) void k_front(const u16* __restrict__ x, char* ws, const u16* __restrict__ logA, const u16* __restrict__ Aim,
                                                  const u16* __restrict__ Cre, const u16* __restrict__ Cim, const u16* __restrict__ logdt,
                                                  const u16* __restrict__ Dskip, const u16* __restrict__ w1 STAMP_ARG) {
    extern __shared__ __attribute__((aligned(16))) char smem[];
    constexpr int ST_FIRST = 0;
    asm volatile(".Lcode_begin:");
    const int tid = threadIdx.x, lane = tid & 63;
    const int wave8 = __builtin_amdgcn_readfirstlane(tid >> 6);
    const int wave = wave8;
    STAMP_INIT();
    asm volatile("" :: "s"(x), "s"(ws), "s"(logA), "s"(Aim), "s"(Cre), "s"(Cim), "s"(logdt), "s"(Dskip));
    const int blk = blockIdx.x, xg = blk & 7, r5 = blk >> 3;
    const bool conv = r5 < 27;
    const int d0 = conv ? (xg * 9 + r5 / 3) * 8 + min(3 * (r5 % 3), 5) : 0;
    u16* u8 = (u16*)(ws + WS_U);
    u16* y8 = (u16*)(ws + WS_Y);
    u32* ep = (u32*)(ws + WS_EP);
    GAS u32* epg = (GAS u32*)ep;
    const int rr = lane & 7, j8 = lane >> 3;
    u32x4 xv[2]; u16 x9[2]; u32 tag; KParam kp, kp2;
    if (wave < 4) {
        const int ro = (blk * 8 + rr) * D_ + wave * 144;
#pragma unroll
        for (int v = 0; v < 2; ++v) { xv[v] = __builtin_nontemporal_load((const u32x4*)(x + ro + 72 * v + j8 * 8)); x9[v] = __builtin_nontemporal_load(x + ro + 72 * v + 64 + j8); }
        tag = __hip_atomic_load(epg, RLX_AGENT) + 1u;
    } else {
        kp = kparam_load(logA, Aim, Cre, Cim, logdt, Dskip, d0 + ((wave >> 1) & 1), lane);
        kp2 = kparam_load(logA, Aim, Cre, Cim, logdt, Dskip, d0 + 2, lane);
    }
    volatile LAS u32* cnt = (volatile LAS u32*)(smem + KA_MISC + 256);
    if (tid < 6) cnt[tid] = 0;
    BAR_L0();
    f32x2* chp = (f32x2*)(smem + KA_MISC + 288);
    const int i16 = lane & 15, g = lane >> 4;
    const u32 lb = lds_addr(smem);
    const char* w1win = (const char*)w1 + ((27 * xg) / 6) * 256;
    if (wave < 4) {
        __builtin_amdgcn_s_setprio(2);
        float a[18];
#pragma unroll
        for (int v = 0; v < 2; ++v) {
#pragma unroll
            for (int i = 0; i < 4; ++i) { a[9 * v + 2 * i] = bf_lo(xv[v][i]); a[9 * v + 2 * i + 1] = bf_hi(xv[v][i]); }
            a[9 * v + 8] = bf2f(x9[v]);
        }
        PIN(a[0]); STAMP0(1);
        float mean;
        const float var = row_stats(a, (f32x2*)(smem + KA_MISC), wave, lane, mean, [&]() { lds_arrive(cnt, lane); lds_wait<0>(cnt, 4u); });
        const float m = rbf(mean);
        const float inv = __builtin_amdgcn_rcpf(rbf(__builtin_amdgcn_sqrtf(rbf(rbf(var) + 1e-5f))));
        const float mi = -m * inv;
        const __amdgpu_buffer_rsrc_t rs_u = __builtin_amdgcn_make_buffer_rsrc(u8, 0, L_ * D_ * 2, 0x00020000);
#pragma unroll
        for (int v = 0; v < 2; ++v) {
            float n[9];
#pragma unroll
            for (int i = 0; i < 9; ++i) n[i] = __builtin_fmaf(a[9 * v + i], inv, mi);
            char* wb = smem + KA_WB + (2 * wave + v) * 1024;
            *(u32x4*)(wb + lane * 16) = u32x4{pk_f16(n[0], n[1]), pk_f16(n[2], n[3]), pk_f16(n[4], n[5]), pk_f16(n[6], n[7])};
            const int uo = (blk * D_ + wave * 144 + 72 * v) * 8;
            __builtin_amdgcn_raw_buffer_store_b16((u16)pk_f16(n[8], n[8]), rs_u, (uo + 512 + lane) * 2, 0, SC1);
            __builtin_amdgcn_raw_buffer_store_b128(tr8x8(wb, lane), rs_u, (uo + lane * 8) * 2, 0, SC1);
        }
        STAMP0(2);
#pragma unroll
        for (int i = 0; i < 5; ++i) { const int o = lane * 16 + i * 1024; if (o < (wave < 3 ? KCH : 1536)) *(u32x4*)(smem + KA_K + wave * KCH + o) = u32x4{0, 0, 0, 0}; }
        WAIT_V(0);
        u32 old = 0;
        if (lane == 0) old = __hip_atomic_fetch_add((LAS u32*)&cnt[1], 1u, __ATOMIC_RELAXED, __HIP_MEMORY_SCOPE_WORKGROUP);
        if (__builtin_amdgcn_readfirstlane(old) == 3u) { if (lane < 8) __hip_atomic_store(epg + 256 + lane * 256 + blk, tag, RLX_AGENT); STAMP(3); }
        __builtin_amdgcn_s_setprio(0);
        if (tid < 24) *(u32x4*)(ws + WS_CTL + blk * 384 + tid * 16) = u32x4{0, 0, 0, 0};
    } else if (!conv) {
        unsigned long long pc; u32 back, len;
        asm volatile("s_getpc_b64 %0\n.Lcode_here:\n\ts_mov_b32 %1, .Lcode_here-.Lcode_begin\n\ts_mov_b32 %2, .Lcode_end-.Lcode_begin" : "=s"(pc), "=s"(back), "=s"(len));
        u32 junk = 0;
        if (wave < 7) junk = *(const GAS u32*)(wave == 6 ? ws + WS_EP + 1024 + lane * 128 : (const char*)(pc - back) + min((u32)((r5 - 27) * 128 + tid - 256) * 128u, len));
        const unsigned long long t0 = __builtin_amdgcn_s_memrealtime();
        while (__builtin_amdgcn_s_memrealtime() - t0 < PFA_DELAY) __builtin_amdgcn_s_sleep(16);
        if (wave == 7) junk = *(const GAS u32*)(ws + min((size_t)lane << 19, WS_END));
        prefetch_pair<10>(lb + KA_JUNK, w1win, F_ * 2, r5 * 256 + tid - 256);
        asm volatile("" :: "v"(junk));
        if (PFA > 0) prefetch_lines<9, false>((const char*)w1 + xg * 1152, F_ * 2, 819 * 9, PFA * 1280, (r5 - 27) * 256 + tid - 256, 1280);
        WAIT_V(0);
        STAMP(12 + (wave & 1));
    } else {
#pragma unroll 1
        for (int it = 0; it < (wave >= 6 ? 2 : 1); ++it) {
            const int c = it ? 2 : ((wave >> 1) & 1);
            table_task(kp, c, wave & 1, smem, chp, lane);
            lds_arrive(cnt + 2 + c, lane);
            kp = kp2;
        }
        if (wave == 4) STAMP(5);
        if (wave == 6) STAMP(6);
    }
    if (!conv) { STAMP_DUMP(); return; }
    if (wave < 3) {
        lds_wait<1>(cnt + 2 + wave, 2u);
        STAMP0(7);
        const u32 kb = lb + KA_K + wave * KCH - 8;
        const u32 ta = lb + wave * TBL + i16 * 64 + ((g ^ ((-(i16 >> 2)) & 3)) << 4);
        const int r1 = min(2 * (i16 >> 2) + (i16 & 3), 8);
        const u32 t1 = lb + wave * TBL + (48 + r1) * 64 + ((g ^ ((-(r1 >> 2)) & 3)) << 4);
        const float dsr = chp[wave][1];
        u32x4 T[8], A0[8], A1[8], AX[8];
        f32x4 a0, a1, ax;
#pragma unroll
        for (int ks = 0; ks < 3; ++ks) { DS_R128(T[ks], ta, ks * TKS); DS_R128(A0[ks], ta, ks * TKS + 1024); DS_R128(A1[ks], t1, ks * TKS); DS_R128(AX[ks], ta, ks * TKS + 2048); }
        WAIT_L0();
#pragma unroll
        for (int ks = 3; ks < 6; ++ks) { DS_R128(T[ks], ta, ks * TKS); DS_R128(A0[ks], ta, ks * TKS + 1024); DS_R128(A1[ks], t1, ks * TKS); DS_R128(AX[ks], ta, ks * TKS + 2048); }
        MFMA_F16_Z(a0, A0[0], T[0]); MFMA_F16_Z(a1, A1[0], T[0]); MFMA_F16_Z(ax, T[0], AX[0]);
#pragma unroll
        for (int ks = 1; ks < 8; ++ks) {
            if (ks == 3) {
                WAIT_L0();
#pragma unroll
                for (int k2 = 6; k2 < 8; ++k2) { DS_R128(T[k2], ta, k2 * TKS); DS_R128(A0[k2], ta, k2 * TKS + 1024); DS_R128(A1[k2], t1, k2 * TKS); DS_R128(AX[k2], ta, k2 * TKS + 2048); }
            }
            if (ks == 6) WAIT_L0();
            MFMA_F16(a0, A0[ks], T[ks]); MFMA_F16(a1, A1[ks], T[ks]); MFMA_F16(ax, T[ks], AX[ks]);
        }
        asm volatile("s_nop 7\n\ts_nop 3" : "+v"(a0), "+v"(a1), "+v"(ax));
        if (lane == 0) a0[0] += dsr;
        const u32 pz = kb + (542 - 16 * i16 - 4 * g) * 2;
        put4<1120>(pz, a0[0], a0[1], a0[2], a0[3]);
        put4<1120>(pz - 512, a1[0], a1[0], a1[1], a1[2]);
        if (lane == 63) put4<1120>(kb + (542 - 512) * 2, a1[2], a1[2], a1[2], 0.f);
        const int ph = i16 < 8 ? (i16 & 3) : 4 + (i16 & 1), a = i16 < 8 ? (i16 >> 2) : ((i16 - 8) >> 1);
        put4<160>(kb + 2240 + ph * 320 + (62 - 16 * a - 4 * g) * 2, ax[0], ax[1], ax[2], ax[3]);
        lds_arrive(cnt + 5, lane);
        if (wave == 2) STAMP(8);
    } else if (wave == 3) {
        const __amdgpu_buffer_rsrc_t rs_f = __builtin_amdgcn_make_buffer_rsrc(ep + 256 + xg * 256, 0, 1024, 0x00020000);
        u32 go[3];
#pragma unroll
        for (int i = 0; i < 3; ++i) { const int e = 64 * i + lane; go[i] = (e / 3) * (D_ * 16) + (e % 3) * 16 + 4096 - 1024 * i; PIN(go[i]); }
        const char* src = (const char*)(u8 + (size_t)d0 * 8) - 4096;
        const unsigned long long t0 = __builtin_amdgcn_s_memrealtime();
        __builtin_amdgcn_s_sleep(POLL1_FIRST);
        for (;;) {
            const u32x4 f = __builtin_amdgcn_raw_buffer_load_b128(rs_f, lane * 16, 0, SC1);
            if (__all(f[0] == tag && f[1] == tag && f[2] == tag && f[3] == tag)) break;
            if (__builtin_amdgcn_s_memrealtime() - t0 > SPIN_LIMIT) break;
            __builtin_amdgcn_s_sleep(POLL1_SLEEP);
        }
#pragma unroll
        for (int i = 0; i < 4; ++i) GLDS3(" sc1", lb + KA_U + 1536 + i * 3072, src + (size_t)i * (64 * D_ * 16), go[0], go[1], go[2]);
        STAMP(9);
        if (blk == 0 && lane == 0) __hip_atomic_store(epg, tag, RLX_AGENT);
        lds_wait<1>(cnt + 5, 3u);
    }
    f32x4* red = (f32x4*)(smem + KA_RED);
    char* ob = smem + KA_OUT;
    if (wave < 4) {
        f32x4 acc[8];
        const char* ka = smem + KA_K + (i16 & 1) * 1120 + (542 - 16 - (i16 & 14) + 8 * g) * 2;
        const u32 ub = lb + KA_U + (30 + 2 * i16 + g) * 48;
        B8 X, Y;
        if (wave < 3) {
            u32x4 A[5][2], A16, A32, T2[2][3], T3[4];
            const char* kc = ka + wave * KCH;
#pragma unroll
            for (int jd = 0; jd < 5; ++jd) { A[jd][0] = ld4(kc - 64 * jd); A[jd][1] = ld4(kc - 64 * (jd + 8)); }
            A16 = ld4(kc - 64 * 16);
            const char* tc = smem + KA_K + wave * KCH + 2240 + (g >> 1) * 320 + (i16 & 1) * 160 + (62 - (i16 & 14) + 8 * (g & 1)) * 2;
#pragma unroll
            for (int b = 0; b < 3; ++b) { T2[0][b] = ld4(tc - 32 * b); T2[1][b] = ld4(tc + 640 - 32 * b); }
#pragma unroll
            for (int b = 0; b < 4; ++b) T3[b] = ld4(tc + 1280 - 32 * b);
            {
                const u32 k1024 = *(const u16*)(smem + KA_K + wave * KCH + 2240 + 3 * 320 + 31 * 2);
#pragma unroll
                for (int w = 0; w < 4; ++w) {
                    const int e = 8 * g + 2 * w - i16;
                    A32[w] = ((e == 12 || e == 13) ? k1024 : 0u) | ((e == 11 || e == 12) ? (k1024 << 16) : 0u);
                }
            }
            const u32 ba = ub + 16 * wave;
            const int q = i16 >> 2, p = lane & 3;
            const u32 sa2 = lb + wave * SCH + (g >> 1) * 3088 + (128 * (g & 1) + 16 * q + 4 * p) * 2;
            const u32 sa3 = lb + wave * SCH + 6176 + (g >> 1) * 2048 + (128 * (g & 1) + 16 * q + 4 * p) * 2;
#pragma unroll
            for (int jd = 0; jd < 5; ++jd) { asm volatile("" : "+v"(A[jd][0]), "+v"(A[jd][1])); }
            asm volatile("" : "+v"(A16), "+v"(A32), "+v"(T3[0]), "+v"(T3[1]), "+v"(T3[2]), "+v"(T3[3]));
            asm volatile("" : "+v"(T2[0][0]), "+v"(T2[0][1]), "+v"(T2[0][2]), "+v"(T2[1][0]), "+v"(T2[1][1]), "+v"(T2[1][2]));
            BAR_L0();
            STAMP0(10); CSTAMP0(30);
#pragma unroll
            for (int m = 0; m < 8; ++m) DS_R128(X.f[m], ba, 1536 * m);
            WAIT_L0();
            cv_dense<1, true>(acc, A[0][0], A[0][1], X, Y, ba - 192);
#pragma unroll
            for (int m = 0; m < 6; ++m) MFMA_F16(acc[m + 2], A16, X.f[m]);
#pragma unroll
            for (int m = 0; m < 4; ++m) MFMA_F16(acc[m + 4], A32, X.f[m]);
            WAIT_L0();
            cv_dense<1>(acc, A[1][0], A[1][1], Y, X, ba - 384); WAIT_L0();
            cv_dense<1>(acc, A[2][0], A[2][1], X, Y, ba - 576); WAIT_L0();
            cv_dense<1>(acc, A[3][0], A[3][1], Y, X, ba - 768);
            BAR_L0();
            cv_dense<2>(acc, A[4][0], A[4][1], X, Y, sa2); WAIT_L0();
            {
                u32x2 h[8][2];
                int cnt = 0;
#pragma unroll
                for (int b = 0; b < 3; ++b)
#pragma unroll
                    for (int k = 0; k < 8; ++k) {
                        const int pr = k >> 2, be = k & 3;
                        MFMA_F16(acc[2 + be + b], Y.f[2 * be + pr], T2[pr][b]);
                        if (cnt < 4) { DS_TR(h[cnt][0], sa2, 512 * (4 + (cnt >> 1)) + 16 * (1 - (cnt & 1))); DS_TR(h[cnt][1], sa2, 512 * (4 + (cnt >> 1)) + 128 + 16 * (1 - (cnt & 1))); }
                        else if (cnt < 8) { DS_TR(h[cnt][0], sa3, 512 * (cnt - 4)); DS_TR(h[cnt][1], sa3, 512 * (cnt - 4) + 128); }
                        ++cnt;
                    }
#pragma unroll
                for (int k = 0; k < 8; ++k) X.f[k] = u32x4{h[k][0][0], h[k][0][1], h[k][1][0], h[k][1][1]};
            }
            WAIT_L0();
            MFMA_F16(acc[6], X.f[0], T2[0][0]); MFMA_F16(acc[7], X.f[2], T2[0][0]);
            MFMA_F16(acc[4], X.f[4], T3[0]); MFMA_F16(acc[5], X.f[5], T3[0]);
            MFMA_F16(acc[6], X.f[1], T2[1][0]); MFMA_F16(acc[7], X.f[3], T2[1][0]);
            MFMA_F16(acc[5], X.f[4], T3[1]);
            MFMA_F16(acc[6], X.f[6], T3[0]); MFMA_F16(acc[7], X.f[0], T2[0][1]);
            MFMA_F16(acc[6], X.f[5], T3[1]); MFMA_F16(acc[7], X.f[1], T2[1][1]);
            MFMA_F16(acc[6], X.f[4], T3[2]); MFMA_F16(acc[7], X.f[7], T3[0]);
            MFMA_F16(acc[7], X.f[6], T3[1]); MFMA_F16(acc[7], X.f[5], T3[2]); MFMA_F16(acc[7], X.f[4], T3[3]);
            FENCE8(acc);
            STAMP0(11); CSTAMP0(31);
            BAR_L0();
            const float scale = chp[wave][0];
#pragma unroll
            for (int t = 0; t < 8; ++t) {
                const f32x4 v = (acc[t] + red[(wave * 8 + t) * 64 + lane]) * scale;
                *(u32x2*)(ob + (32 * t + 2 * i16 + (g >> 1)) * 48 + wave * 16 + (g & 1) * 8) = u32x2{pk_bf16(v[0], v[1]), pk_bf16(v[2], v[3])};
            }
        } else {
            u32x4 A[3][3][2];
            f32x4 acc2[8];
#pragma unroll
            for (int c = 0; c < 3; ++c)
#pragma unroll
                for (int k = 0; k < 3; ++k) { A[c][k][0] = ld4(ka + c * KCH - 64 * (5 + k)); A[c][k][1] = ld4(ka + c * KCH - 64 * (13 + k)); }
#pragma unroll
            for (int c = 0; c < 3; ++c) asm volatile("" : "+v"(A[c][0][0]), "+v"(A[c][0][1]), "+v"(A[c][1][0]), "+v"(A[c][1][1]), "+v"(A[c][2][0]), "+v"(A[c][2][1]));
            const u32 ba = ub - 192 * 5, ra = lb + KA_RED + lane * 16;
            STAMP(19);
            WAIT_V(0);
            STAMP(20);
            BAR_L0();
#pragma unroll
            for (int m = 0; m < 8; ++m) DS_R128(X.f[m], ba, 1536 * m);
            WAIT_L0();
            cv_dense<1, true>(acc, A[0][0][0], A[0][0][1], X, Y, ba - 192); WAIT_L0();
            cv_dense<1>(acc, A[0][1][0], A[0][1][1], Y, X, ba - 384); WAIT_L0();
            cv_dense<1>(acc, A[0][2][0], A[0][2][1], X, Y, ba + 16); WAIT_L0();
            cv_dense<1, true, 0>(acc2, A[1][0][0], A[1][0][1], Y, X, ba + 16 - 192, acc, ra);
            BAR_L0();
            cv_dense<1>(acc2, A[1][1][0], A[1][1][1], X, Y, ba + 16 - 384); WAIT_L0();
            cv_dense<1>(acc2, A[1][2][0], A[1][2][1], Y, X, ba + 32); WAIT_L0();
            cv_dense<1, true, 1>(acc, A[2][0][0], A[2][0][1], X, Y, ba + 32 - 192, acc2, ra); WAIT_L0();
            cv_dense<1>(acc, A[2][1][0], A[2][1][1], Y, X, ba + 32 - 384); WAIT_L0();
            cv_dense<0>(acc, A[2][2][0], A[2][2][1], X, Y, 0);
            asm volatile("s_nop 1");
#pragma unroll
            for (int t = 0; t < 8; ++t) DS_W128(ra, acc[t], (16 + t) * 1024);
            STAMP(16);
            BAR_L0();
        }
    } else {
        BAR_L0();
        auto pool = [&](int c, int n, bool far) {
            const char* up = smem + KA_U + (29 + n) * 48 + 16 * c;
            u32 w[16], od[14], t2[14], v4[13];
#pragma unroll
            for (int k = 0; k < 4; ++k) { const u32x4 v = *(const u32x4*)(up + 48 * k); w[4 * k] = v[0]; w[4 * k + 1] = v[1]; w[4 * k + 2] = v[2]; w[4 * k + 3] = v[3]; }
#pragma unroll
            for (int k = 2; k < 14; ++k) od[k] = __builtin_amdgcn_alignbit(w[k + 1], w[k], 16);
#pragma unroll
            for (int k = 3; k < 14; ++k) t2[k] = pk_add_h(w[k], od[k - 1]);
#pragma unroll
            for (int k = 4; k < 13; ++k) v4[k] = pk_add_h(t2[k], t2[k - 1]);
            u32x4 s1, s2, so, se;
#pragma unroll
            for (int o = 0; o < 4; ++o) {
                s1[o] = pk_add_h(od[10 + o], t2[10 + o]);
                s2[o] = pk_add_h(v4[9 + o], w[7 + o]);
                so[o] = pk_add_h(pk_add_h(v4[9 + o], t2[7 + o]), od[9 + o]);
                se[o] = pk_add_h(pk_add_h(v4[6 + o], v4[4 + o]), w[2 + o]);
            }
            char* sp = smem + c * SCH;
            *(u32x4*)(sp + 16 + n * 16) = s1;
            *(u32x4*)(sp + 3088 + 16 + n * 16) = s2;
            if (far) { *(u32x4*)(sp + 6176 + n * 16) = so; *(u32x4*)(sp + 8224 + n * 16) = se; }
        };
        if (wave < 7) { pool(wave - 4, lane, true); pool(wave - 4, 64 + lane, true); }
        else {
            pool(0, 128 + lane, false); pool(1, 128 + lane, false); pool(2, 128 + lane, false);
            if (lane < 6) *(u32x4*)(smem + (lane >> 1) * SCH + (lane & 1) * 3088) = u32x4{0, 0, 0, 0};
        }
        if (wave == 4) STAMP(17);
        BAR_L0();
        prefetch_pair<10>(lb + KA_JUNK, w1win, F_ * 2, r5 * 256 + tid - 256);
        if (wave == 6) STAMP(18);
        BAR_L0();
    }
    BAR_L0();
#pragma unroll
    for (int it = 0; it < 2; ++it) {
        const int e = tid + 512 * it, n = e / 3, ch = e - 3 * n;
        if (e < 768) *(u32x4*)(y8 + ((size_t)n * D_ + d0 + ch) * 8) = *(const u32x4*)(ob + e * 16);
    }
    STAMP0(15);
    WAIT_V(0);
    STAMP_DUMP();
}

DEV f32x2 gelu2(f32x2 v) {
    const float k1 = -2.0f * 0.7978845608028654f * 1.4426950408889634f;
    const f32x2 t = v * __builtin_elementwise_fma(v * v, f32x2{0.044715f * k1, 0.044715f * k1}, f32x2{k1, k1});
    const f32x2 e = f32x2{__builtin_amdgcn_exp2f(t[0]), __builtin_amdgcn_exp2f(t[1])} + 1.0f;
    return v * f32x2{__builtin_amdgcn_rcpf(e[0]), __builtin_amdgcn_rcpf(e[1])};
}
#define W_POL ""
struct G1Set { u32x4 W[4]; u32x4 H[3]; };
template <int SLOT, int KS, bool RD>
DEV void g1_half(f32x4 (&acc)[4][3], const G1Set& cur, G1Set& nxt, const u32 (&wa)[4][2], u32 ha) {
    constexpr int WO = SLOT * 16384 + KS * 8192, HO = SLOT * 12288 + KS * 6144;
    u32x2 w[4][2];
#pragma unroll
    for (int i = 0; i < 12; ++i) {
        MFMA_BF(acc[i / 3][i % 3], cur.W[i / 3], cur.H[i % 3]);
        if (RD) {
            if (i == 0) { DS_R128(nxt.H[0], ha, HO); DS_TR(w[0][0], wa[0][0], WO); }
            if (i == 1) { DS_TR(w[0][1], wa[0][1], WO); DS_R128(nxt.H[1], ha, HO + 256); }
            if (i == 2) { DS_R128(nxt.H[2], ha, HO + 512); }
            if (i == 3) { DS_TR(w[1][0], wa[1][0], WO); DS_TR(w[1][1], wa[1][1], WO); }
            if (i == 4) { DS_TR(w[2][0], wa[2][0], WO); DS_TR(w[2][1], wa[2][1], WO); }
            if (i == 5) { DS_TR(w[3][0], wa[3][0], WO); DS_TR(w[3][1], wa[3][1], WO); }
        }
    }
    if (RD) {
#pragma unroll
        for (int t = 0; t < 4; ++t) nxt.W[t] = u32x4{w[t][0][0], w[t][0][1], w[t][1][0], w[t][1][1]};
    }
}

constexpr int G2_GS = 18432;
constexpr int G2_LDS = 65536 + 4 * G2_GS;
constexpr int SLAB = 9216;
constexpr int KB_JUNK = G2_LDS + 512;
constexpr int KB_LDS = KB_JUNK + 256;
struct G2Set { u32x4 W[2]; u32x4 A[9]; };
template <int SLOT, int KS, bool RD>
DEV void g2_half(f32x4 (&acc)[9][2], const G2Set& cur, G2Set& nxt, const u32 (&wa)[2][2], u32 ga0, u32 ga2) {
    constexpr int WO = SLOT * 16384 + KS * 8192, GO = (SLOT & 1) * G2_GS + KS * 9216;
    const u32 ga = SLOT >= 2 ? ga2 : ga0;
    u32x2 w[2][2];
#pragma unroll
    for (int i = 0; i < 18; ++i) {
        MFMA_BF(acc[i / 2][i % 2], cur.A[i / 2], cur.W[i % 2]);
        if (RD) {
            if (i == 0) DS_R128(nxt.A[0], ga, GO);
            if (i == 1) { DS_TR(w[0][0], wa[0][0], WO); DS_TR(w[0][1], wa[0][1], WO); }
            if (i == 2) { DS_TR(w[1][0], wa[1][0], WO); DS_TR(w[1][1], wa[1][1], WO); }
            if (i >= 3 && i <= 10) DS_R128(nxt.A[i - 2], ga, GO + (i - 2) * 256);
        }
    }
    if (RD) {
#pragma unroll
        for (int t = 0; t < 2; ++t) nxt.W[t] = u32x4{w[t][0][0], w[t][0][1], w[t][1][0], w[t][1][1]};
    }
}
#define PFM 11264
#define W2_POL ""
__global__ __launch_bounds__(512, 2) void k_back(const u16* __restrict__ x, const u16* __restrict__ w1, const u16* __restrict__ b1,
                                                 const u16* __restrict__ w2, const u16* __restrict__ b2, u16* __restrict__ out, char* ws STAMP_ARG) {
    extern __shared__ __attribute__((aligned(16))) char smem[];
    const u16* y8 = (const u16*)(ws + WS_Y);
    u16* h = (u16*)(ws + WS_H); u16* hh8 = (u16*)(ws + WS_HH); u16* G8 = (u16*)(ws + WS_G);
    char* slabs = ws + WS_SLAB; u32* ctl = (u32*)(ws + WS_CTL);
    constexpr int ST_FIRST = 32;
    const int lane = threadIdx.x & 63;
    const int wave8 = __builtin_amdgcn_readfirstlane(threadIdx.x >> 6);
    const int wave = wave8 & 3;
    STAMP_INIT();
    asm volatile("" :: "s"(x), "s"(w1), "s"(b1), "s"(w2), "s"(b2), "s"(out), "s"(ws));
    const int xg = blockIdx.x & 7, r5 = blockIdx.x >> 3;
    const u32 lbase = lds_addr(smem);
    const int g = lane >> 4, q = (lane & 15) >> 2, p = lane & 3, i16 = lane & 15;
    GAS u32* ctlg = (GAS u32*)ctl;
    const __amdgpu_buffer_rsrc_t rs_h = __builtin_amdgcn_make_buffer_rsrc(h, 0, L_ * D_ * 2, 0x00020000);
    if (wave8 < 4) {
        const int blk = blockIdx.x, rr = lane & 7, j = lane >> 3;
        const int ro = (blk * 8 + rr) * D_ + wave * 144;
        const int uo = (blk * D_ + wave * 144) * 8;
        u32x4 yu[2], xv[2]; u16 y9[2], x9[2];
#pragma unroll
        for (int v = 0; v < 2; ++v) {
            yu[v] = *(const u32x4*)(y8 + uo + 576 * v + lane * 8);
            xv[v] = __builtin_nontemporal_load((const u32x4*)(x + ro + 72 * v + j * 8));
            y9[v] = y8[uo + 576 * v + 512 + lane]; x9[v] = __builtin_nontemporal_load(x + ro + 72 * v + 64 + j);
        }
        char* wb = smem + 131072 + wave * 2048;
        float a[18];
#pragma unroll
        for (int v = 0; v < 2; ++v) {
            *(u32x4*)(wb + v * 1024 + lane * 16) = yu[v];
            const u32x4 yv = tr8x8(wb + v * 1024, lane);
            u32x4 hv;
#pragma unroll
            for (int i = 0; i < 4; ++i) {
                hv[i] = pk_bf16(bf_lo(xv[v][i]) + bf_lo(yv[i]), bf_hi(xv[v][i]) + bf_hi(yv[i]));
                a[9 * v + 2 * i] = bf_lo(hv[i]); a[9 * v + 2 * i + 1] = bf_hi(hv[i]);
            }
            a[9 * v + 8] = rbf(bf2f(x9[v]) + bf2f(y9[v]));
            char* hb = smem + 120832 + (rr * D_ + wave * 144 + 72 * v) * 2;
            *(u32x4*)(hb + j * 16) = hv;
            *(u16*)(hb + 128 + j * 2) = (u16)(__float_as_uint(a[9 * v + 8]) >> 16);
        }
        STAMP0(33);
        float mean;
        const float var = row_stats(a, (f32x2*)(smem + 139264), wave, lane, mean, [&]() { BAR_L0(); });
        const float r1 = __builtin_amdgcn_rsqf(var + 1e-5f);
        const float inv = r1 * __builtin_amdgcn_rsqf(var * r1 * r1 + 1e-5f);
        const float mi = -mean * inv;
        const __amdgpu_buffer_rsrc_t rs_hh = __builtin_amdgcn_make_buffer_rsrc(hh8, 0, L_ * D_ * 2, 0x00020000);
#pragma unroll
        for (int v = 0; v < 2; ++v) {
            float n[9];
#pragma unroll
            for (int i = 0; i < 9; ++i) n[i] = __builtin_fmaf(a[9 * v + i], inv, mi);
            *(u32x4*)(wb + v * 1024 + lane * 16) = u32x4{pk_bf16(n[0], n[1]), pk_bf16(n[2], n[3]), pk_bf16(n[4], n[5]), pk_bf16(n[6], n[7])};
            __builtin_amdgcn_raw_buffer_store_b16((u16)pk_bf16(n[8], n[8]), rs_hh, (uo + 576 * v + 512 + lane) * 2, 0, SC1);
            __builtin_amdgcn_raw_buffer_store_b128(tr8x8(wb + v * 1024, lane), rs_hh, (uo + 576 * v + lane * 8) * 2, 0, SC1);
        }
#pragma unroll
        for (int i = 0; i < 3; ++i) {
            const int o = (i * 256 + (int)threadIdx.x) * 16;
            if (i < 2 || wave == 0) __builtin_amdgcn_raw_buffer_store_b128(*(const u32x4*)(smem + 120832 + o), rs_h, blk * 9216 + o, 0, SC1);
        }
        STAMP0(34);
        WAIT_V(0);
        BAR();
        if (wave8 == 0 && lane < 32) __hip_atomic_fetch_add(ctlg + CTL_E3 + (lane * 8 + (blk >> 5)) * 32, 1u, RLX_AGENT);
        STAMP0(35);
    }
    const int kq = xg >> 1, tile = (xg & 1) * 32 + r5;
    const int s0 = (tile >> 2) * 128, d2 = (tile & 3) * 144;
    const char* w2win = (const char*)w2 + (size_t)kq * (1152 * L_ * 2) + (xg & 1) * 2048;
    const bool g1 = r5 < 27;
    const int id = xg * 27 + r5;
    const int fg = id / 6, mt1 = id % 6;
    const int f0 = fg * 128, d0 = mt1 * 96;
    if (wave8 >= 4) {
        u32 woff[4];
        const char* w2src = (const char*)(w2 + s0) + (size_t)kq * (1152 * L_ * 2) - 4096;
        auto woffs = [&](int pitch) {
#pragma unroll
            for (int i = 0; i < 4; ++i) {
                int row = 16 * wave + 4 * i + (lane >> 4), c = lane & 15;
                int swz = ((row & 3) << 2) | ((row >> 2) & 3);
                woff[i] = row * pitch + ((c ^ swz) << 4) + 4096 - 1024 * i;
            }
        };
        auto issue_w = [&](const char* ws, int slot) { GLDS4(W_POL, lbase + slot * 16384 + 4096 * wave, ws, woff[0], woff[1], woff[2], woff[3]); };
        auto issue_w2 = [&](const char* ws, int slot) { GLDS4(W2_POL, lbase + slot * 16384 + 4096 * wave, ws, woff[0], woff[1], woff[2], woff[3]); };
        if (g1) {
            u32 hoff[3];
            woffs(F_ * 2);
#pragma unroll
            for (int i = 0; i < 3; ++i) {
                int un = 192 * wave + 64 * i + lane;
                hoff[i] = (un / 96) * (D_ * 16) + (un % 96) * 16 + 4096 - 1024 * i;
            }
            const char* wsrc = (const char*)(w1 + f0) - 4096;
            const char* hsrc = (const char*)(hh8 + (size_t)d0 * 8) - 4096;
            auto issue_h = [&](int kt) { GLDS3(" sc1", lbase + 65536 + (kt & 3) * 12288 + 3072 * wave, hsrc + (size_t)kt * (8 * D_ * 16), hoff[0], hoff[1], hoff[2]); };
            auto issue = [&](int kt) { issue_w(wsrc + (size_t)kt * (64 * F_ * 2), kt & 3); issue_h(kt); };
#pragma unroll
            for (int kt = 0; kt < 2; ++kt) issue_w(wsrc + (size_t)kt * (64 * F_ * 2), kt);
            BAR();
            BAR();
#pragma unroll
            for (int kt = 2; kt < 4; ++kt) issue_w(wsrc + (size_t)kt * (64 * F_ * 2), kt);
            BAR();
            issue_h(0); issue_h(1); issue_h(2); issue_h(3);
            WAIT_V(9); BAR();
            WAIT_V(6); BAR(); issue(4);
            WAIT_V(10); BAR(); issue(5);
            for (int kt = 3; kt < 29; ++kt) {
                WAIT_V(14);
                BAR();
                issue(kt + 3);
            }
            woffs(L_ * 2);
            WAIT_V(14); BAR(); issue_w2(w2src, 0);
            WAIT_V(11); BAR(); issue_w2(w2src + 1 * (64 * L_ * 2), 1);
            WAIT_V(8); BAR(); issue_w2(w2src + 2 * (64 * L_ * 2), 2);
            BAR(); issue_w2(w2src + 3 * (64 * L_ * 2), 3);
        } else {
            woffs(L_ * 2);
            const int t = (r5 - 27) * 256 + (threadIdx.x - 256);
            constexpr int P0 = 819 * 9 + PFA * 1280;
            if (PFB > 0) prefetch_lines<9, false>((const char*)w1 + xg * 1152, F_ * 2, P0, 1280, t, 1280);
            BAR();
            if (PFB > 1) prefetch_lines<9, false>((const char*)w1 + xg * 1152, F_ * 2, P0 + 1280, 1280, t, 1280);
            BAR();
            prefetch_lines<9, false>((const char*)w1 + xg * 1152, F_ * 2, P0 + PFB * 1280, 18432 - P0 - PFB * 1280, t, 1280);
            if (wave8 == 4) STAMP(49);
            prefetch_lines<16, false>(w2win, L_ * 2, 0, 4096, t, 1280);
            if (wave8 == 4) STAMP(48);
            prefetch_lines<16, false>(w2win, L_ * 2, 4096 + PFM, 18432 - 4096 - PFM, t, 1280);
            if (wave8 == 4) STAMP(50);
#pragma unroll
            for (int kt = 0; kt < 4; ++kt) issue_w2(w2src + (size_t)kt * (64 * L_ * 2), kt);
        }
        if (wave8 == 4) STAMP(51);
        u32 goff[5];
#pragma unroll
        for (int i = 0; i < 5; ++i) {
            int un = min(64 * i + lane, 287);
            goff[i] = (2 * wave + un / 144) * (D_ * 16) + (un % 144) * 16 + 4096 - 1024 * (i & 3);
        }
        const char* gsrc = (const char*)(G8 + (size_t)d2 * 8) + (size_t)kq * (144 * D_ * 16) - 4096;
        auto issue_g = [&](int kt) {
            const char* gs = gsrc + (size_t)kt * (8 * D_ * 16);
            const u32 gd = lbase + 65536 + (kt & 3) * G2_GS + 4608 * wave;
            GLDS4(" sc1", gd, gs, goff[0], goff[1], goff[2], goff[3]);
            if (lane < 32) GLDS1(" sc1", gd + 4096, gs, goff[4]);
        };
        auto issue2 = [&](int kt) { issue_w2(w2src + (size_t)kt * (64 * L_ * 2), kt & 3); issue_g(kt); };
        if (wave8 == 4) {
            const unsigned long long t0 = __builtin_amdgcn_s_memrealtime();
            GAS u32* c = ctlg + CTL_E4 + (((kq * 4 + (tile & 3)) * 4 + ((tile >> 2) & 3)) * 4 + (lane & 3)) * 32;
            for (;;) {
                int v = (int)__hip_atomic_load(c, RLX_AGENT);
                v += __builtin_amdgcn_update_dpp(0, v, 0xB1, 0xf, 0xf, true);
                v += __builtin_amdgcn_update_dpp(0, v, 0x4E, 0xf, 0xf, true);
                if (__builtin_amdgcn_readfirstlane(v) >= 54) break;
                if (__builtin_amdgcn_s_memrealtime() - t0 > SPIN_LIMIT) break;
                __builtin_amdgcn_s_sleep(2);
            }
        }
        asm volatile("" ::: "memory");
        BAR();
        issue_g(0); issue_g(1); issue_g(2); issue_g(3);
        WAIT_V(15); BAR();
        WAIT_V(10); BAR(); issue2(4);
        WAIT_V(14); BAR(); issue2(5);
        for (int kt = 3; kt < 15; ++kt) {
            WAIT_V(18);
            BAR();
            issue2(kt + 3);
        }
        WAIT_V(18); BAR();
        WAIT_V(9); BAR();
        WAIT_V(0); BAR();
    } else if (g1) {
        const int wf = wave >> 1, wd = wave & 1;
        u32 wa[4][2];
#pragma unroll
        for (int t = 0; t < 4; ++t)
#pragma unroll
            for (int tau = 0; tau < 2; ++tau) {
                int row = 8 * g + 4 * tau + q;
                int swz = (q << 2) | ((2 * g + tau) & 3);
                int ch = 2 * (4 * wf + t) + (p >> 1);
                wa[t][tau] = lbase + row * 256 + ((ch ^ swz) << 4) + 8 * (p & 1);
            }
        const u32 ha = lbase + 65536 + g * 1536 + (48 * wd + i16) * 16;
        f32x4 acc[4][3];
#pragma unroll
        for (int t = 0; t < 4; ++t) {
            const u32x2 bw = *(const u32x2*)(b1 + f0 + 64 * wf + 16 * t + 4 * g);
#pragma unroll
            for (int m = 0; m < 3; ++m) { acc[t][m] = f32x4{bf_lo(bw[0]), bf_hi(bw[0]), bf_lo(bw[1]), bf_hi(bw[1])}; PIN(acc[t][m]); }
        }
        G1Set X, Y;
#pragma unroll
        for (int t = 0; t < 4; ++t) { Y.W[t] = u32x4{0, 0, 0, 0}; asm volatile("" : "+v"(Y.W[t])); }
#pragma unroll
        for (int m = 0; m < 3; ++m) { Y.H[m] = u32x4{0, 0, 0, 0}; asm volatile("" : "+v"(Y.H[m])); }
        if (wave8 == 0) {
            const unsigned long long t0 = __builtin_amdgcn_s_memrealtime();
            GAS u32* c = ctlg + CTL_E3 + ((blockIdx.x & 31) * 8 + (lane & 7)) * 32;
            for (;;) {
                u32 v = __hip_atomic_load(c, RLX_AGENT);
                if (__all(v >= 32u)) break;
                if (__builtin_amdgcn_s_memrealtime() - t0 > SPIN_LIMIT) break;
                __builtin_amdgcn_s_sleep(1);
            }
        }
        STAMP0(36);
        asm volatile("" ::: "memory");
        BAR();
        asm volatile("s_nop 1");
        for (int kt = 0; kt < 32; kt += 4) {
            BAR_L0();
            if (kt == 0) { STAMP0(37); CSTAMP0(60); }
            g1_half<0, 0, true>(acc, Y, X, wa, ha); WAIT_L0();
            g1_half<0, 1, true>(acc, X, Y, wa, ha);
            BAR_L0();
            g1_half<1, 0, true>(acc, Y, X, wa, ha); WAIT_L0();
            g1_half<1, 1, true>(acc, X, Y, wa, ha);
            BAR_L0();
            g1_half<2, 0, true>(acc, Y, X, wa, ha); WAIT_L0();
            g1_half<2, 1, true>(acc, X, Y, wa, ha);
            BAR_L0();
            g1_half<3, 0, true>(acc, Y, X, wa, ha); WAIT_L0();
            g1_half<3, 1, true>(acc, X, Y, wa, ha);
        }
        BAR_L0();
        g1_half<0, 0, false>(acc, Y, X, wa, ha);
        FENCE6(acc[2][0], acc[2][1], acc[2][2], acc[3][0], acc[3][1], acc[3][2]);
#pragma unroll
        for (int t = 0; t < 2; ++t)
#pragma unroll
            for (int m = 0; m < 3; ++m) PIN(acc[t][m]);
        STAMP0(38); CSTAMP0(61);
        const __amdgpu_buffer_rsrc_t rs_g = __builtin_amdgcn_make_buffer_rsrc(G8, 0, D_ * F_ * 2, 0x00020000);
        const int go = (((f0 + 64 * wf) / 8 + 2 * (g & 1) + (g >> 1)) * D_ + d0 + 48 * wd + i16) * 16;
#pragma unroll
        for (int t = 0; t < 4; t += 2) {
#pragma unroll
            for (int m = 0; m < 3; ++m) {
                u32 o[2][2];
#pragma unroll
                for (int k = 0; k < 2; ++k) {
                    const f32x4 v = acc[t + k][m];
                    const f32x2 lo = gelu2(f32x2{v[0], v[1]}), hi = gelu2(f32x2{v[2], v[3]});
                    o[k][0] = pk_bf16(lo[0], lo[1]);
                    o[k][1] = pk_bf16(hi[0], hi[1]);
                }
                auto r0 = __builtin_amdgcn_permlane16_swap(o[0][0], o[1][0], false, false);
                auto r1 = __builtin_amdgcn_permlane16_swap(o[0][1], o[1][1], false, false);
                __builtin_amdgcn_raw_buffer_store_b128(u32x4{r0[0], r1[0], r0[1], r1[1]}, rs_g, go + (2 * t * D_ + 16 * m) * 16, 0, SC1);
            }
        }
        STAMP0(39);
        WAIT_V(0);
        if (lane < 4) __hip_atomic_fetch_add(ctlg + CTL_E4 + ((((fg / 9) * 4 + (2 * mt1 + wd) / 3) * 4 + lane) * 4 + (fg & 3)) * 32, 1u, RLX_AGENT);
        STAMP0(40);
    }
    const __amdgpu_buffer_rsrc_t rs_s = __builtin_amdgcn_make_buffer_rsrc(slabs + (size_t)tile * (16 * SLAB), 0, 16 * SLAB, 0x00020000);
    GAS u32* fl = ctlg + CTL_XF + tile * 128;
    const int s = s0 + 32 * kq + i16;
    u32x2 hv[2][2];
    float bb[2];
    auto load_res = [&]() {
#pragma unroll
        for (int j = 0; j < 2; ++j)
#pragma unroll
            for (int t = 0; t < 2; ++t)
                hv[j][t] = __builtin_amdgcn_raw_buffer_load_b64(rs_h, ((s + 16 * t) * D_ + d2 + 16 * (j ? 8 : wave8) + 4 * g) * 2, 0, SC1);
        bb[0] = bf2f(b2[s]); bb[1] = bf2f(b2[s + 16]);
    };
    if (wave8 < 4) {
        if (false) {
            constexpr int PW = (PFM + 107) / 108;
            prefetch_pair<16>(lbase + KB_JUNK, w2win, L_ * 2, 4096 + min((r5 * 4 + wave) * PW + lane, PFM - 1));
            if (lane < PW - 64) prefetch_pair<16>(lbase + KB_JUNK, w2win, L_ * 2, 4096 + min((r5 * 4 + wave) * PW + 64 + lane, PFM - 1));
        }
        u32 wa[2][2];
#pragma unroll
        for (int t = 0; t < 2; ++t)
#pragma unroll
            for (int tau = 0; tau < 2; ++tau) {
                int row = 8 * g + 4 * tau + q;
                int swz = (q << 2) | ((2 * g + tau) & 3);
                int ch = 2 * (2 * wave + t) + (p >> 1);
                wa[t][tau] = lbase + row * 256 + ((ch ^ swz) << 4) + 8 * (p & 1);
            }
        const u32 ga0 = lbase + 65536 + g * 2304 + i16 * 16, ga2 = ga0 + 2 * G2_GS;
        f32x4 acc[9][2];
#pragma unroll
        for (int m = 0; m < 9; ++m)
#pragma unroll
            for (int t = 0; t < 2; ++t) { acc[m][t] = f32x4{0, 0, 0, 0}; asm volatile("" : "+v"(acc[m][t])); }
        G2Set X, Y;
#pragma unroll
        for (int t = 0; t < 2; ++t) { Y.W[t] = u32x4{0, 0, 0, 0}; asm volatile("" : "+v"(Y.W[t])); }
#pragma unroll
        for (int m = 0; m < 9; ++m) { Y.A[m] = u32x4{0, 0, 0, 0}; asm volatile("" : "+v"(Y.A[m])); }
        asm volatile("" ::: "memory");
        BAR();
        asm volatile("" ::: "memory");
        STAMP0(41);
        load_res();
        asm volatile("s_nop 1");
        for (int kt = 0; kt < 16; kt += 4) {
            BAR_L0();
            if (kt == 0) { STAMP0(42); CSTAMP0(62); }
            g2_half<0, 0, true>(acc, Y, X, wa, ga0, ga2); WAIT_L0();
            g2_half<0, 1, true>(acc, X, Y, wa, ga0, ga2);
            BAR_L0();
            g2_half<1, 0, true>(acc, Y, X, wa, ga0, ga2); WAIT_L0();
            g2_half<1, 1, true>(acc, X, Y, wa, ga0, ga2);
            BAR_L0();
            g2_half<2, 0, true>(acc, Y, X, wa, ga0, ga2); WAIT_L0();
            g2_half<2, 1, true>(acc, X, Y, wa, ga0, ga2);
            BAR_L0();
            g2_half<3, 0, true>(acc, Y, X, wa, ga0, ga2); WAIT_L0();
            g2_half<3, 1, true>(acc, X, Y, wa, ga0, ga2);
        }
        BAR_L0();
        g2_half<0, 0, true>(acc, Y, X, wa, ga0, ga2); WAIT_L0();
        g2_half<0, 1, true>(acc, X, Y, wa, ga0, ga2);
        BAR_L0();
        g2_half<1, 0, true>(acc, Y, X, wa, ga0, ga2); WAIT_L0();
        g2_half<1, 1, true>(acc, X, Y, wa, ga0, ga2);
        WAIT_L0();
        g2_half<0, 0, false>(acc, Y, X, wa, ga0, ga2);
        FENCE6(acc[6][0], acc[6][1], acc[7][0], acc[7][1], acc[8][0], acc[8][1]);
#pragma unroll
        for (int m = 0; m < 6; ++m)
#pragma unroll
            for (int t = 0; t < 2; ++t) PIN(acc[m][t]);
        STAMP0(43); CSTAMP0(63);
        if (wave == kq) {
            f32x4* own = (f32x4*)(smem + 32768);
#pragma unroll
            for (int i = 0; i < 18; ++i) own[i * 64 + lane] = acc[i >> 1][i & 1];
        } else {
            const int so = (wave * 4 + kq) * SLAB + lane * 16;
#pragma unroll
            for (int m = 0; m < 9; ++m)
                __builtin_amdgcn_raw_buffer_store_b128(u32x4{pk_bf16(acc[m][0][0], acc[m][0][1]), pk_bf16(acc[m][0][2], acc[m][0][3]),
                                                             pk_bf16(acc[m][1][0], acc[m][1][1]), pk_bf16(acc[m][1][2], acc[m][1][3])}, rs_s, so + m * 1024, 0, SC1);
            WAIT_V(0);
            if (lane == 0) __hip_atomic_store(fl + wave * 32 + kq, 1u, RLX_AGENT);
        }
        if (wave == ((kq + 1) & 3)) STAMP(44);
    } else {
        load_res();
        if (wave8 == 4) {
            const unsigned long long t0 = __builtin_amdgcn_s_memrealtime();
            for (;;) {
                u32 f = __hip_atomic_load(fl + kq * 32 + (lane & 3), RLX_AGENT);
                if (__all(f == 1u || (lane & 3) == kq)) break;
                if (__builtin_amdgcn_s_memrealtime() - t0 > SPIN_LIMIT) break;
                __builtin_amdgcn_s_sleep(2);
            }
        }
    }
    BAR_L0();
    STAMP0(45);
    u32x4 part[2][3];
#pragma unroll
    for (int j = 0; j < 2; ++j) {
        const int po = j == 0 ? wave8 * 1024 + lane * 16 : (wave8 == 0 ? 8192 + lane * 16 : 0x40000000);
#pragma unroll
        for (int k = 0; k < 3; ++k) part[j][k] = __builtin_amdgcn_raw_buffer_load_b128(rs_s, (kq * 4 + ((kq + 1 + k) & 3)) * SLAB + po, 0, SC1);
    }
#pragma unroll
    for (int j = 0; j < 2; ++j) {
        if (j == 1 && wave8 != 0) break;
        const int m = j ? 8 : wave8;
#pragma unroll
        for (int t = 0; t < 2; ++t) {
            f32x4 v = ((const f32x4*)(smem + 32768))[(m * 2 + t) * 64 + lane];
#pragma unroll
            for (int k = 0; k < 3; ++k) v += f32x4{bf_lo(part[j][k][2 * t]), bf_hi(part[j][k][2 * t]), bf_lo(part[j][k][2 * t + 1]), bf_hi(part[j][k][2 * t + 1])};
            const u32 y0 = pk_bf16(v[0] + bb[t], v[1] + bb[t]), y1 = pk_bf16(v[2] + bb[t], v[3] + bb[t]);
            *(u32x2*)(out + (size_t)(s + 16 * t) * D_ + d2 + 16 * m + 4 * g) =
                u32x2{pk_bf16(bf_lo(hv[j][t][0]) + bf_lo(y0), bf_hi(hv[j][t][0]) + bf_hi(y0)), pk_bf16(bf_lo(hv[j][t][1]) + bf_lo(y1), bf_hi(hv[j][t][1]) + bf_hi(y1))};
        }
    }
    STAMP0(47);
    STAMP_DUMP();
    asm volatile(".Lcode_end:");
}

extern "C" void kernel_launch(void* const* d_in, const int* in_sizes, int n_in,
                              void* d_out, int out_size, void* d_ws, size_t ws_size,
                              hipStream_t stream) {
    const u16* x = (const u16*)d_in[0];
    const u16* logA = (const u16*)d_in[1];
    const u16* Aim = (const u16*)d_in[2];
    const u16* Cre = (const u16*)d_in[3];
    const u16* Cim = (const u16*)d_in[4];
    const u16* logdt = (const u16*)d_in[5];
    const u16* Dskip = (const u16*)d_in[6];
    const u16* w1 = (const u16*)d_in[8];
    const u16* b1 = (const u16*)d_in[9];
    const u16* w2 = (const u16*)d_in[10];
    const u16* b2 = (const u16*)d_in[11];
    char* ws = (char*)d_ws;
    (void)hipFuncSetAttribute((const void*)k_front, hipFuncAttributeMaxDynamicSharedMemorySize, LDS_REQ(KA_LDS));
    (void)hipFuncSetAttribute((const void*)k_back, hipFuncAttributeMaxDynamicSharedMemorySize, LDS_REQ(KB_LDS));
    k_front<<<256, 512, LDS_REQ(KA_LDS), stream>>>(x, ws, logA, Aim, Cre, Cim, logdt, Dskip, w1 STAMP_PASS);
    k_back<<<256, 512, LDS_REQ(KB_LDS), stream>>>(x, w1, b1, w2, b2, (u16*)d_out, ws STAMP_PASS);
}
```

```cpp
#include <hip/hip_runtime.h>
#include <stdint.h>

typedef unsigned short u16;
typedef unsigned int u32;
typedef short s16x4 __attribute__((ext_vector_type(4)));
typedef short s16x8 __attribute__((ext_vector_type(8)));
typedef __bf16 bf16x8 __attribute__((ext_vector_type(8)));
typedef __bf16 bf16x2 __attribute__((ext_vector_type(2)));
typedef _Float16 f16x8 __attribute__((ext_vector_type(8)));
typedef _Float16 f16x2 __attribute__((ext_vector_type(2)));
typedef float f32x4 __attribute__((ext_vector_type(4)));
typedef float f32x2 __attribute__((ext_vector_type(2)));
typedef u32 u32x4 __attribute__((ext_vector_type(4)));
typedef u32 u32x2 __attribute__((ext_vector_type(2)));
#define LAS __attribute__((address_space(3)))
#define DEV __device__ __forceinline__

constexpr int L_ = 2048, D_ = 576, N_ = 128, F_ = 4608;

DEV float bf_lo(u32 w) { return __uint_as_float(w << 16); }
DEV float bf_hi(u32 w) { return __uint_as_float(w & 0xffff0000u); }
DEV float bf2f(u16 v) { return __uint_as_float(((u32)v) << 16); }
DEV u32 pk_bf16(float a, float b) { f32x2 v = {a, b}; return __builtin_bit_cast(u32, __builtin_convertvector(v, bf16x2)); }
DEV u32 pk_f16(float a, float b) { f32x2 v = {a, b}; return __builtin_bit_cast(u32, __builtin_convertvector(v, f16x2)); }
DEV float rbf(float a) { return bf_lo(pk_bf16(a, a)); }

DEV u32 clean(u32 v) { return v < 0xfffffffeu ? v : 0xfffffffeu; }
DEV u32x4 clean4(u32x4 v) { return u32x4{clean(v[0]), clean(v[1]), clean(v[2]), clean(v[3])}; }
DEV u32 max3(u32 a, u32 b, u32 c) { const u32 m = a > b ? a : b; return m > c ? m : c; }
DEV u32 max5(u32 m, u32x4 v) { return max3(max3(m, v[0], v[1]), v[2], v[3]); }

DEV u32 lds_addr(const void* p) { return (u32)(uintptr_t)(LAS const char*)p; }
template <int CTRL> DEV float dpp_get(float v) { return __builtin_bit_cast(float, __builtin_amdgcn_update_dpp(0, __builtin_bit_cast(int, v), CTRL, 0xf, 0xf, true)); }
DEV float swap32_add(float v) {
    auto r = __builtin_amdgcn_permlane32_swap(__float_as_uint(v), __float_as_uint(v), false, false);
    return __uint_as_float(r[0]) + __uint_as_float(r[1]);
}
DEV float swap16_add(float v) {
    auto r = __builtin_amdgcn_permlane16_swap(__float_as_uint(v), __float_as_uint(v), false, false);
    return __uint_as_float(r[0]) + __uint_as_float(r[1]);
}
DEV float sum_j(float v) { return swap32_add(swap16_add(v + dpp_get<0x128>(v))); }
DEV float sum_j4(float v) { return swap16_add(v + dpp_get<0x128>(v)); }
DEV u32 umax(u32 a, u32 b) { return a > b ? a : b; }
template <int CTRL> DEV u32 dpp_umax(u32 v) { return umax(v, (u32)__builtin_amdgcn_update_dpp(0, (int)v, CTRL, 0xf, 0xf, true)); }
DEV u32 wave_umax(u32 v) {
    v = dpp_umax<0xB1>(v); v = dpp_umax<0x4E>(v); v = dpp_umax<0x141>(v); v = dpp_umax<0x140>(v);
    { auto r = __builtin_amdgcn_permlane16_swap(v, v, false, false); v = umax(r[0], r[1]); }
    { auto r = __builtin_amdgcn_permlane32_swap(v, v, false, false); v = umax(r[0], r[1]); }
    return v;
}
DEV u32x4 tr8x8(const char* wbuf, int lane) {
    const int q = (lane & 15) >> 2, p = lane & 3;
    const char* a = wbuf + ((lane >> 4) * 16 + (p >> 1) * 8 + q) * 16 + (p & 1) * 8;
    s16x4 lo = __builtin_amdgcn_ds_read_tr16_b64_v4i16((LAS s16x4*)a);
    s16x4 hi = __builtin_amdgcn_ds_read_tr16_b64_v4i16((LAS s16x4*)(a + 64));
    return __builtin_bit_cast(u32x4, __builtin_shufflevector(lo, hi, 0, 1, 2, 3, 4, 5, 6, 7));
}

#define MFMA_BF(acc, a, b) asm volatile("v_mfma_f32_16x16x32_bf16 %0, %1, %2, %0" : "+v"(acc) : "v"(a), "v"(b))
#define MFMA_F16(acc, a, b) asm volatile("v_mfma_f32_16x16x32_f16 %0, %1, %2, %0" : "+v"(acc) : "v"(a), "v"(b))
#define DS_R128(dst, addr, off) asm volatile("ds_read_b128 %0, %1 offset:%2" : "=v"(dst) : "v"(addr), "i"(off))
#define DS_TR(dst, addr, off) asm volatile("ds_read_b64_tr_b16 %0, %1 offset:%2" : "=v"(dst) : "v"(addr), "i"(off))
#define WAIT_L0() asm volatile("s_waitcnt lgkmcnt(0)" ::: "memory")
#define WAIT_V(n) asm volatile("s_waitcnt vmcnt(" #n ")" ::: "memory")
#define BAR_L0() asm volatile("s_waitcnt lgkmcnt(0)\n\ts_barrier" ::: "memory")
#define BAR() asm volatile("s_barrier" ::: "memory")
DEV float add3v(float a, float b, float c) { float r; asm volatile("v_add_f32 %0, %1, %2\n\tv_add_f32 %0, %0, %3" : "=&v"(r) : "v"(a), "v"(b), "v"(c)); return r; }
#define PIN(x) asm volatile("" : "+v"(x))
#define FENCE6(a0, a1, a2, a3, a4, a5) asm volatile("s_nop 7\n\ts_nop 3" : "+v"(a0), "+v"(a1), "+v"(a2), "+v"(a3), "+v"(a4), "+v"(a5) :: "memory")
#define GLDS4(POL, lds, base, o0, o1, o2, o3) asm volatile("s_mov_b32 m0, %0\n\ts_nop 0\n\t" \
    "global_load_lds_dwordx4 %2, %1" POL "\n\tglobal_load_lds_dwordx4 %3, %1 offset:1024" POL "\n\t" \
    "global_load_lds_dwordx4 %4, %1 offset:2048" POL "\n\tglobal_load_lds_dwordx4 %5, %1 offset:3072" POL \
    :: "s"(lds), "s"(base), "v"(o0), "v"(o1), "v"(o2), "v"(o3) : "memory")
#define GLDS3(POL, lds, base, o0, o1, o2) asm volatile("s_mov_b32 m0, %0\n\ts_nop 0\n\t" \
    "global_load_lds_dwordx4 %2, %1" POL "\n\tglobal_load_lds_dwordx4 %3, %1 offset:1024" POL "\n\t" \
    "global_load_lds_dwordx4 %4, %1 offset:2048" POL \
    :: "s"(lds), "s"(base), "v"(o0), "v"(o1), "v"(o2) : "memory")
#define GLDS1(POL, lds, base, o0) asm volatile("s_mov_b32 m0, %0\n\ts_nop 0\n\t" \
    "global_load_lds_dwordx4 %2, %1" POL :: "s"(lds), "s"(base), "v"(o0) : "memory")

#define GAS __attribute__((address_space(1)))
#define RLX_AGENT __ATOMIC_RELAXED, __HIP_MEMORY_SCOPE_AGENT
constexpr int SC1 = 16;
DEV void st128(u32x4 v, __amdgpu_buffer_rsrc_t rs, int vo, int so) {
    asm("" : "+v"(v));
    __builtin_amdgcn_raw_buffer_store_b128(v, rs, vo, so, SC1);
    asm volatile("s_nop 1" :: "v"(v) : "memory");
}
constexpr unsigned long long SPIN_LIMIT = 2000000ull;
constexpr int HINT_T = 3072, HINT_G = 3328, HINT_G2 = 3584;
constexpr int CTL_E3 = 0, CTL_E4 = 8192, CTL_XF = 16384;

constexpr int UP = 11264;
DEV int u_line(int j) { return ((j & 7) == 3 ? 72 + (j >> 3) + (j >= 24) : j) * 128; }
DEV int u_unit(int c) { return u_line(c >> 3) + (c & 7) * 16; }
constexpr size_t SZ = (size_t)L_ * D_ * 2;
constexpr size_t WS_CTL = 0, WS_EP = 98304, WS_FL = 131072, WS_U = 262144, WS_Y = WS_U + 256 * UP, WS_HH = WS_Y + 216 * 16384, WS_G = WS_HH + SZ,
                 WS_SLAB = WS_G + (size_t)D_ * F_ * 2, WS_END = WS_SLAB + (size_t)64 * 16 * 9216;
DEV int y_msg(int n, int grp) { return grp * 16384 + ((n >> 4) * 8 + (n & 7)) * 128 + ((n >> 3) & 1) * 64; }
static_assert(WS_END < (64u << 19), "the translation warm-up covers 64 x 512 KB");

#define PREFETCH(POL, lds_junk, base, off) asm volatile("s_mov_b32 m0, %0\n\ts_nop 0\n\tglobal_load_lds_dword %1, %2" POL "\n\ts_waitcnt vmcnt(1)" :: "s"(lds_junk), "v"(off), "s"(base) : "memory")

DEV void lds_bump(u32 a, u32 one) { asm volatile("s_mov_b64 exec, 1\n\tds_add_u32 %0, %1\n\ts_mov_b64 exec, -1" :: "v"(a), "v"(one) : "memory"); }
DEV void lds_arrive(volatile LAS u32* c, int) { lds_bump((u32)(uintptr_t)c, 1u); }
template <int SLEEP> DEV void lds_wait(volatile LAS u32* c, u32 n) { while (__builtin_amdgcn_readfirstlane(*c) < n) { if (SLEEP) __builtin_amdgcn_s_sleep(SLEEP); } asm volatile("" ::: "memory"); }

template <int NH, class SYNC>
DEV float row_stats(const float (&a)[9 * NH], f32x2* red, int slot, int lane, float& mean_out, SYNC sync) {
    float s0 = a[0] + a[1], s1 = a[2];
#pragma unroll
    for (int i = 3; i < 9 * NH; ++i) { if (i & 1) s1 += a[i]; else s0 += a[i]; }
    const float mw = sum_j(s0 + s1) * (1.0f / (72 * NH));
    float q0 = 0, q1 = 0;
#pragma unroll
    for (int i = 0; i < 9 * NH; ++i) { const float d = a[i] - mw; if (i & 1) q1 = __builtin_fmaf(d, d, q1); else q0 = __builtin_fmaf(d, d, q0); }
    const float q = sum_j(q0 + q1);
    if (lane < 8) red[slot * 8 + lane] = f32x2{mw, q};
    sync();
    const f32x2 pj = red[NH == 2 ? lane & 31 : lane];
    const float mean = (NH == 2 ? sum_j4(pj[0]) : sum_j(pj[0])) * (NH / 8.0f);
    const float dj = pj[0] - mean;
    const float t = __builtin_fmaf((72.0f * NH) * dj, dj, pj[1]);
    mean_out = mean;
    return (NH == 2 ? sum_j4(t) : sum_j(t)) * (1.0f / D_);
}
template <int V> struct IC { static constexpr int v = V; };

typedef f32x2 cf;
DEV cf cmul(cf p, cf q) {
    cf t, r;
    asm("v_pk_mul_f32 %0, %1, %2 op_sel:[0,0] op_sel_hi:[0,1]" : "=v"(t) : "v"(p), "v"(q));
    asm("v_pk_fma_f32 %0, %1, %2, %3 op_sel:[1,1,0] op_sel_hi:[1,0,1] neg_lo:[1,0,0]" : "=v"(r) : "v"(p), "v"(q), "v"(t));
    return r;
}
DEV u32 hmul(u32 p, u32 q) {
    u32 t, r;
    asm("v_pk_mul_f16 %0, %1, %2 op_sel:[0,0] op_sel_hi:[0,1]" : "=v"(t) : "v"(p), "v"(q));
    asm("v_pk_fma_f16 %0, %1, %2, %3 op_sel:[1,1,0] op_sel_hi:[1,0,1] neg_lo:[1,0,0]" : "=v"(r) : "v"(p), "v"(q), "v"(t));
    return r;
}
DEV u32 toh(cf v) { return pk_f16(v[0], v[1]); }
DEV cf cexp_s(float zr2, float zi, float s) {
    const float mag = __builtin_amdgcn_exp2f(zr2 * s);
    const float th = zi * s;
    const float c_hi = 0.15915494309189535f, c_lo = 6.4206382e-9f;
    const float hi = th * c_hi;
    const float lo = __builtin_fmaf(th, c_hi, -hi) + th * c_lo;
    const float rev = __builtin_amdgcn_fractf(hi) + lo;
    return cf{mag * __builtin_amdgcn_cosf(rev), mag * __builtin_amdgcn_sinf(rev)};
}
constexpr int TKS = 57 * 64;
constexpr int TBL = 8 * TKS;
constexpr int SCH = 2 * 3088 + 2 * 2048;
constexpr int KA_TAB = 32768;
constexpr int KA_SIG = KA_TAB, KA_RED = 65536, KA_OUT = KA_RED + 24576;
constexpr int KA_K = KA_TAB + 3 * TBL;
constexpr int KCH = 4736;
constexpr int KA_U = KA_K + 3 * KCH;
constexpr int KA_WB = KA_U + 288 * 48;
constexpr int KA_MISC = KA_WB + 8192;
constexpr int KB_RED = KA_MISC + 512;
constexpr int KA_JUNK = KB_RED + 512;
constexpr int KB_BIAS = KA_JUNK + 256;
constexpr int ALL_LDS = KB_BIAS + 512;
constexpr int KB_WB = KA_WB;
static_assert(KA_SIG + 3 * SCH <= KA_RED && KA_OUT + 24576 <= KA_K && KA_K % 16 == 0 && KA_MISC % 16 == 0 && ALL_LDS <= 159 * 1024, "LDS map");

DEV u32 pk_add_h(u32 a, u32 b) { return __builtin_bit_cast(u32, __builtin_bit_cast(f16x2, a) + __builtin_bit_cast(f16x2, b)); }
template <int CP>
DEV void put4(u32 p, float v0, float v1, float v2, float v3) {
    const u32 p32 = pk_f16(v3, v2), p10 = pk_f16(v1, v0), p21 = pk_f16(v2, v1);
    asm volatile("ds_write_b64 %0, %1 offset:%4\n\tds_write_b16 %0, %5 offset:2\n\tds_write_b32 %0, %2 offset:4\n\tds_write_b16_d16_hi %0, %3 offset:8"
                 :: "v"(p), "v"(u32x2{p32, p10}), "v"(p21), "v"(p10), "i"(CP + 4), "v"(p32) : "memory");
}
#define MFMA_F16_Z(acc, a, b) asm volatile("v_mfma_f32_16x16x32_f16 %0, %1, %2, 0" : "=&v"(acc) : "v"(a), "v"(b))
#define DS_W128(addr, src, off) asm volatile("ds_write_b128 %0, %1 offset:%2" :: "v"(addr), "v"(src), "i"(off) : "memory")
struct Frag { u32x2 lo, hi; DEV u32x4 v() const { return u32x4{lo[0], lo[1], hi[0], hi[1]}; } };
template <int DW> DEV void frag_ld(Frag& f, u32 a) {
    asm volatile("ds_read2_b32 %0, %1 offset0:%2 offset1:%3" : "=v"(f.lo) : "v"(a), "i"(DW), "i"(DW + 1));
    asm volatile("ds_read2_b32 %0, %1 offset0:%2 offset1:%3" : "=v"(f.hi) : "v"(a), "i"(DW + 2), "i"(DW + 3));
}
DEV void ds_r64(f32x2& d, u32 a, int off) { asm volatile("ds_read_b64 %0, %1 offset:%2" : "=v"(d) : "v"(a), "i"(off)); }
DEV void ds_r128(u32x4& d, u32 a, int off) { DS_R128(d, a, off); }
DEV void ds_w128(u32 a, const f32x4& v, int off) { DS_W128(a, v, off); }
template <int M0, int M1, bool ZERO, int K0, class SIDE>
DEV void cv_part(f32x4* a, const Frag (&F)[2], const u32x4* x, SIDE side) {
    const u32x4 A0 = F[0].v(), A1 = F[1].v();
    int k = K0;
#pragma unroll
    for (int m = M0; m < M1; ++m) { if (ZERO) MFMA_F16_Z(a[m], A0, x[m]); else MFMA_F16(a[m], A0, x[m]); side(k++); }
#pragma unroll
    for (int m = M0 ? M0 : 1; m < M1; ++m) { MFMA_F16(a[m], A1, x[m - 1]); side(k++); }
}
template <int J> DEV void frag_pair(Frag (&F)[2], u32 kc) {
    if (J == 0) frag_ld<0>(F[0], kc + 1024); else frag_ld<256 - 16 * (J ? J : 1)>(F[0], kc);
    frag_ld<256 - 16 * (J + 8)>(F[1], kc);
}
typedef u32 u32_a2 __attribute__((aligned(2)));
struct KParam { u32 la, ai, cr, ci; u16 dt, ds; };
DEV KParam kparam_load(const u16* logA, const u16* Aim, const u16* Cre, const u16* Cim, const u16* logdt, const u16* Dskip, int d, int lane) {
    const int o = d * N_ + 2 * lane;
    return KParam{__builtin_nontemporal_load((const u32*)(logA + o)), __builtin_nontemporal_load((const u32*)(Aim + o)),
                  __builtin_nontemporal_load((const u32*)(Cre + o)), __builtin_nontemporal_load((const u32*)(Cim + o)),
                  __builtin_nontemporal_load(logdt + d), __builtin_nontemporal_load(Dskip + d)};
}
struct TabCtx { float dt, zr, zi; char* tb; int i16; };
DEV TabCtx tab_ctx(u32 la, u32 ai, u16 ldt, int c, int hf, char* smem, int lane) {
    const int sh = hf ? 0 : 16;
    const float dt = rbf(__expf(bf2f(ldt)));
    return TabCtx{dt, -(rbf(__expf(bf_hi(la << sh))) * dt) * 1.4426950408889634f, bf_hi(ai << sh) * dt, smem + KA_TAB + c * TBL + (hf * 4 + (lane >> 4)) * TKS, lane & 15};
}
DEV void tab_put(const TabCtx& t, int row, u32 v) { *(u32*)(t.tb + row * 64 + ((t.i16 ^ (((-(row >> 2)) & 3) << 2)) << 2)) = v; }
DEV void table_t2(const u32 (&la)[3], const u32 (&ai)[3], const u16 (&ldt)[3], int hf, char* smem, int lane) {
    TabCtx t[3]; u32 wf[3][4], wq[3][4];
#pragma unroll
    for (int c = 0; c < 3; ++c) {
        t[c] = tab_ctx(la[c], ai[c], ldt[c], c, hf, smem, lane);
        const cf w1 = cexp_s(t[c].zr, -t[c].zi, 16.f);
        const cf w2 = cmul(w1, w1), w3 = cmul(w2, w1), w4 = cmul(w2, w2), w8 = cmul(w4, w4), w12 = cmul(w8, w4);
        wf[c][0] = 0x00003c00u; wf[c][1] = toh(w1); wf[c][2] = toh(w2); wf[c][3] = toh(w3);
        wq[c][0] = 0; wq[c][1] = toh(w4); wq[c][2] = toh(w8); wq[c][3] = toh(w12);
    }
#pragma unroll
    for (int c = 0; c < 3; ++c)
#pragma unroll
        for (int a = 0; a < 4; ++a)
#pragma unroll
            for (int b = 0; b < 4; ++b) tab_put(t[c], 4 * a + b, a == 0 ? wf[c][b] : b == 0 ? wq[c][a] : hmul(wq[c][a], wf[c][b]));
}
DEV void table_r(const KParam& k, int c, int hf, char* smem, f32x2* chp, int lane) {
    const int sh = hf ? 0 : 16;
    const TabCtx t = tab_ctx(k.la, k.ai, k.dt, c, hf, smem, lane);
    const float dt = t.dt, dsk = bf2f(k.ds);
    const float cs = __uint_as_float(wave_umax(umax(umax(k.cr & 0x7fff0000u, (k.cr << 16) & 0x7fff0000u), umax(k.ci & 0x7fff0000u, (k.ci << 16) & 0x7fff0000u))));
    const float ksc = fmaxf(fmaxf(2.0f * dt * cs, fabsf(dsk) * (1.0f / 1024)), 1e-30f);
    const float rs = __builtin_amdgcn_rcpf(ksc);
    const cf C = cf{bf_hi(k.cr << sh), bf_hi(k.ci << sh)} * (2.0f * dt * rs);
    auto put = [&](int row, u32 v) { tab_put(t, row, v); };
    const cf e1 = cexp_s(t.zr, t.zi, 1.f), g1 = cexp_s(t.zr, t.zi, 256.f);
    const cf e2 = cmul(e1, e1), e3 = cmul(e2, e1), e4 = cmul(e2, e2);
    const u32 f[4] = {0, toh(e1), toh(e2), toh(e3)};
    u32 cq[5], gk[8];
    { cf v = C; cq[0] = toh(v);
#pragma unroll
      for (int a = 1; a < 5; ++a) { v = cmul(v, e4); cq[a] = toh(v); } }
    { cf v = g1; gk[1] = toh(v);
#pragma unroll
      for (int a = 2; a < 8; ++a) { v = cmul(v, g1); gk[a] = toh(v); } }
#pragma unroll
    for (int a = 0; a < 4; ++a)
#pragma unroll
        for (int b = 0; b < 4; ++b) {
            const u32 r0 = b == 0 ? cq[a] : hmul(cq[a], f[b]);
            put(16 + 4 * a + b, r0);
            if ((b & 1) == 0) put(48 + 2 * a + (b >> 1), hmul(r0, gk[1]));
        }
    put(56, hmul(cq[4], gk[1]));
#pragma unroll
    for (int i = 0; i < 4; ++i) {
        put(32 + i, hmul(cq[i + 1], gk[2])); put(36 + i, hmul(cq[i + 1], gk[3]));
        put(40 + 2 * i, hmul(cq[2], gk[4 + i])); put(41 + 2 * i, hmul(cq[4], gk[4 + i]));
    }
    if (lane == 0 && hf == 0) chp[c] = f32x2{ksc, dsk * rs};
}

DEV f32x2 gelu2(f32x2 v) {
    const float k1 = -2.0f * 0.7978845608028654f * 1.4426950408889634f;
    const f32x2 t = v * __builtin_elementwise_fma(v * v, f32x2{0.044715f * k1, 0.044715f * k1}, f32x2{k1, k1});
    const f32x2 e = f32x2{__builtin_amdgcn_exp2f(t[0]), __builtin_amdgcn_exp2f(t[1])} + 1.0f;
    return v * f32x2{__builtin_amdgcn_rcpf(e[0]), __builtin_amdgcn_rcpf(e[1])};
}
DEV void w_offs(u32 (&woff)[4], int wave, int lane, int pitch) {
#pragma unroll
    for (int i = 0; i < 4; ++i) {
        const int row = 16 * wave + 4 * i + (lane >> 4), c = lane & 15;
        const int swz = ((row & 3) << 2) | ((row >> 2) & 3);
        woff[i] = row * pitch + ((c ^ swz) << 4) + 4096 - 1024 * i;
    }
}
struct G1Set { u32x4 W[4]; u32x4 H[3]; };
template <int SLOT, int KS, bool MM = true, bool RD = true>
DEV void g1_half(f32x4 (&acc)[4][3], const G1Set& cur, G1Set& nxt, const u32 (&wa)[4][2], u32 ha) {
    constexpr int WO = SLOT * 16384 + KS * 8192, HO = SLOT * 12288 + KS * 6144;
    u32x2 w[4][2];
#pragma unroll
    for (int i = 0; i < 12; ++i) {
        if (MM) MFMA_BF(acc[i / 3][i % 3], cur.W[i / 3], cur.H[i % 3]);
        if (RD) {
            if (i == 0) { DS_R128(nxt.H[0], ha, HO); DS_TR(w[0][0], wa[0][0], WO); }
            if (i == 1) { DS_TR(w[0][1], wa[0][1], WO); DS_R128(nxt.H[1], ha, HO + 256); }
            if (i == 2) { DS_R128(nxt.H[2], ha, HO + 512); }
            if (i == 3) { DS_TR(w[1][0], wa[1][0], WO); DS_TR(w[1][1], wa[1][1], WO); }
            if (i == 4) { DS_TR(w[2][0], wa[2][0], WO); DS_TR(w[2][1], wa[2][1], WO); }
            if (i == 5) { DS_TR(w[3][0], wa[3][0], WO); DS_TR(w[3][1], wa[3][1], WO); }
        }
    }
    if (RD) {
#pragma unroll
        for (int t = 0; t < 4; ++t) nxt.W[t] = u32x4{w[t][0][0], w[t][0][1], w[t][1][0], w[t][1][1]};
    }
}

struct G1Stage { u32x2 w[2][4][2]; u32x4 H[2][3]; DEV u32x4 W(int ks, int t) const { return u32x4{w[ks][t][0][0], w[ks][t][0][1], w[ks][t][1][0], w[ks][t][1][1]}; } };
template <int SLOT> DEV void g1_take(G1Stage& n, int i, const u32 (&wa)[4][2], u32 ha) {
    constexpr int WO = SLOT * 16384, HO = SLOT * 12288;
    if (i < 3) { DS_R128(n.H[0][i], ha, HO + 256 * i); DS_R128(n.H[1][i], ha, HO + 6144 + 256 * i); }
    else if (i < 11) { const int t = (i - 3) >> 1, ks = (i - 3) & 1; DS_TR(n.w[ks][t][0], wa[t][0], WO + ks * 8192); DS_TR(n.w[ks][t][1], wa[t][1], WO + ks * 8192); }
}
template <int T0, class SIDE> DEV void g1_part(f32x4 (&acc)[4][3], const G1Stage& c, SIDE side) {
#pragma unroll
    for (int i = 0; i < 12; ++i) { MFMA_BF(acc[T0 + (i % 6) / 3][i % 3], c.W(i / 6, T0 + (i % 6) / 3), c.H[i / 6][i % 3]); side(i); }
}

constexpr int G2_GS = 18432;
constexpr int G2_LDS = 65536 + 4 * G2_GS;
constexpr int KB_RES = 65536 + 2 * G2_GS, KB_RX = G2_LDS;
constexpr int SLAB = 9216;
static_assert(KB_RX + 12288 <= KA_MISC, "LDS map");
struct G2Set { u32x4 W[2]; u32x4 A[9]; };
template <int SLOT, int KS, bool RD, int MODE = 0>
DEV void g2_half(f32x4 (&acc)[9][2], const G2Set& cur, G2Set& nxt, const u32 (&wa)[2][2], u32 ga0, u32 ga2, f32x2* chk = nullptr) {
    constexpr int WO = SLOT * 16384 + KS * 8192, GO = (SLOT & 1) * G2_GS + KS * 9216;
    const u32 ga = SLOT >= 2 ? ga2 : ga0;
    u32x2 w[2][2];
#pragma unroll
    for (int i = 0; i < 18; ++i) {
        if (MODE == 2) asm volatile("v_mfma_f32_16x16x32_bf16 %0, %1, %2, 0" : "=&v"(acc[i / 2][i % 2]) : "v"(cur.A[i / 2]), "v"(cur.W[i % 2]));
        else if (MODE != 1) MFMA_BF(acc[i / 2][i % 2], cur.A[i / 2], cur.W[i % 2]);
        if (MODE == 3) { const f32x4& a = acc[(i / 2 + 4) % 9][0]; asm volatile("v_pk_add_f32 %0, %0, %1" : "+v"(*chk) : "v"(f32x2{a[2 * (i & 1)], a[2 * (i & 1) + 1]})); }
        if (RD) {
            if (i == 0) DS_R128(nxt.A[0], ga, GO);
            if (i == 1) { DS_TR(w[0][0], wa[0][0], WO); DS_TR(w[0][1], wa[0][1], WO); }
            if (i == 2) { DS_TR(w[1][0], wa[1][0], WO); DS_TR(w[1][1], wa[1][1], WO); }
            if (i >= 3 && i <= 10) DS_R128(nxt.A[i - 2], ga, GO + (i - 2) * 256);
        }
    }
    if (RD) {
#pragma unroll
        for (int t = 0; t < 2; ++t) nxt.W[t] = u32x4{w[t][0][0], w[t][0][1], w[t][1][0], w[t][1][1]};
    }
}
constexpr int POLL1_FIRST = 13;
constexpr int Y_DELAY = 14, YL_DELAY = 2, YE_DELAY = 6;
constexpr int GM_DELAY = 8;
constexpr int G_DELAY = 18;
constexpr int XF_GAP = 2;
constexpr int PACE_A = -80, PACE_B = 80;
constexpr int SPARE_Y = 560;
constexpr int STOP_DT = 420;
constexpr int PFA_DELAY = 150;
constexpr int CP0 = 2, CP1 = 4, CP2 = 1;
constexpr int SLAG = 4;
constexpr int HINT_KT = 16, PUBU = 1, H_RETRY = 4, POLL1_SLEEP = 2;
constexpr int PB1 = 108 * CP1, PB0 = PB1 + 108 * CP0;
constexpr int NV = 27, NVG = 9;
constexpr int FS = 16, NREP = 8;
constexpr int E3R = 8;
__global__ __launch_bounds__(512, 2) void k_all(const u16* __restrict__ x, char* ws, const u16* __restrict__ logA, const u16* __restrict__ Aim,
                                                const u16* __restrict__ Cre, const u16* __restrict__ Cim, const u16* __restrict__ logdt,
                                                const u16* __restrict__ Dskip, const u16* __restrict__ w1, const u16* __restrict__ b1,
                                                const u16* __restrict__ w2, const u16* __restrict__ b2, u16* __restrict__ out) {
    extern __shared__ __attribute__((aligned(16))) char smem[];
    const int tid = threadIdx.x, lane = tid & 63;
    const int wave8 = __builtin_amdgcn_readfirstlane(tid >> 6);
    if (wave8 == 7) {
        unsigned long long pc; u32 len;
        asm volatile("s_getpc_b64 %0\n.Lcode_here:\n\ts_mov_b32 %1, .Lcode_end-.Lcode_here" : "=s"(pc), "=s"(len));
        const u32 off = min((u32)((lane & 31) * 32 + (blockIdx.x >> 3)) * 128u, len);
        asm volatile("s_mov_b32 m0, %0\n\ts_nop 0\n\tglobal_load_lds_dword %1, %2" :: "s"(lds_addr(smem) + KA_JUNK), "v"(off), "s"(pc) : "memory");
    }
    asm volatile("" :: "s"(x), "s"(ws), "s"(logA), "s"(Aim), "s"(Cre), "s"(Cim), "s"(logdt), "s"(Dskip), "s"(w1), "s"(b1), "s"(w2), "s"(b2), "s"(out));
    const int blk = blockIdx.x, xg = blk & 7, r5 = blk >> 3;
    const bool conv = r5 < 27;
    const int d0 = conv ? (xg * 9 + r5 / 3) * 8 + min(3 * (r5 % 3), 5) : 0;
    const __amdgpu_buffer_rsrc_t rs_ws = __builtin_amdgcn_make_buffer_rsrc(ws, 0, WS_SLAB, 0x00020000);
    u32* ep = (u32*)(ws + WS_EP);
    GAS u32* epg = (GAS u32*)ep;
    const int rr = lane & 7, j8 = lane >> 3;
    u32 yi[4]; u32x2 y1[2][4];
    const int sub9 = j8 < 3 ? 0 : j8 < 6 ? 1 : 2;
    auto y_off = [&](int h, int k) { return y_msg(blk, h * 27 + (k < 3 ? 3 * j8 + k : 24 + sub9)) + rr * 8; };
#define Y_LOAD(HEAD, TAIL, sel) asm volatile(HEAD "global_load_dwordx2 %0, %4, %8 sc1\n\tglobal_load_dwordx2 %1, %5, %8 sc1\n\tglobal_load_dwordx2 %2, %6, %8 sc1\n\tglobal_load_dwordx2 %3, %7, %8 sc1" TAIL \
        : "+v"(y1[0][0]), "+v"(y1[0][1]), "+v"(y1[0][2]), "+v"(y1[0][3]) : "v"(yi[0]), "v"(yi[1]), "v"(yi[2]), "v"(yi[3]), "s"(ws), "s"(sel) : "memory", "scc")
#define Y_WAIT(HEAD, n, TAIL) asm volatile(HEAD "s_waitcnt vmcnt(%4)" TAIL : "+v"(y1[0][0]), "+v"(y1[0][1]), "+v"(y1[0][2]), "+v"(y1[0][3]) : "i"(n), "s"(blk) : "memory", "scc")
    int esel = blk | (wave8 & 4) << 4;
    asm volatile("" : "+s"(esel));
    u32 so, yo;
    u32 ytag; float sc[3];
    auto y_prep = [&]() {
        ytag = *(const u32*)(smem + KA_MISC + 256 + 19 * 4);
#pragma unroll
        for (int c = 0; c < 3; ++c) sc[c] = *(const float*)(smem + KA_MISC + 288 + 8 * c);
        asm volatile("" : "+v"(ytag), "+v"(sc[0]), "+v"(sc[1]), "+v"(sc[2]));
    };
    auto publish = [&](auto i0, auto i1, auto step, int base = (int)WS_Y) {
        constexpr int I0 = decltype(i0)::v, ST = decltype(step)::v, NP = (decltype(i1)::v - I0 + ST - 1) / ST;
        f32x2 v[NP][3], v2[NP][3];
        const u32 sb = lds_addr(smem) + so, sx = lds_addr(smem) + (so ^ 128);
#pragma unroll
        for (int k = 0; k < NP; ++k) {
            const int i = I0 + k * ST, o = ((i + (i & 1)) / 2) * 1024;
#pragma unroll
            for (int ch = 0; ch < 3; ++ch) {
                ds_r64(v[k][ch], (i & 1) ? sx : sb, o + 24576 + ch * 8192);
                if (ST > 1) ds_r64(v2[k][ch], sb, o + ch * 8192);
            }
        }
        WAIT_L0();
#pragma unroll
        for (int k = 0; k < NP; ++k)
#pragma unroll
            for (int ch = 0; ch < 3; ++ch) { PIN(v[k][ch]); if (ST > 1) { PIN(v2[k][ch]); v[k][ch] += v2[k][ch]; } }
#pragma unroll
        for (int k = 0; k < NP; ++k) {
            u32 c[3];
#pragma unroll
            for (int ch = 0; ch < 3; ++ch) { const f32x2 w = v[k][ch] * sc[ch]; c[ch] = pk_bf16(w[0], w[1]); }
            st128(u32x4{__builtin_amdgcn_perm(c[1], c[0], 0x05040100), __builtin_amdgcn_perm(ytag, c[2], 0x05040100),
                        __builtin_amdgcn_perm(c[1], c[0], 0x07060302), __builtin_amdgcn_perm(ytag, c[2], 0x07060302)}, rs_ws, yo, (k ? (int)WS_Y : base) + (I0 + k * ST) * 1024);
        }
    };
    auto h_marks = [&]() {
#pragma unroll
        for (int i = 0; i < (72 * NV + 863) / 864; ++i) {
            const int jw = (xg * 27 + r5) * 4 + wave8 - 4 + 864 * i;
            st128(u32x4{~0u, ~0u, ~0u, ~0u}, rs_ws, jw < 72 * NV ? lane * 16 : 0x40000000, (int)WS_HH + jw * 1024);
        }
    };
    const bool early = blk < 64;
    auto g_marks = [&]() {
        static_assert(288 * NVG % 864 == 0, "marks");
#pragma unroll
        for (int i = 0; i < 288 * NVG / 864; ++i) {
            const int jg = (xg * 27 + r5) * 4 + (wave8 & 3) + 864 * i;
            st128(u32x4{~0u, ~0u, ~0u, ~0u}, rs_ws, lane * 16, (int)WS_G + (jg / (72 * NVG)) * (144 * 9216) + (jg % (72 * NVG)) * 1024);
        }
    };
    u32x4 xv[2]; u32 x9[2]; u32 tag; KParam kp, kp2; u32 la1, ai1; u32 dt12; u32 woff[4], hoff[3], woff2[4], goff[5], ro[7];
    auto cpf_off = [&](u32 p0, u32 e) {
        const u32 pos = p0 + (e >> 2) * 2, row = ((pos % 576) >> 5) * 64 + (pos / 576) * 32 + (pos & 31);
        return ((e & 3) * 1152 + row) * (L_ * 2) + lane * 128;
    };
    auto cpf_go = [&](u32 o) { asm volatile("s_mov_b32 m0, %0\n\ts_nop 0\n\tglobal_load_lds_dword %1, %2 sc0 sc1" :: "s"(lds_addr(smem) + KA_JUNK), "v"(o), "s"(w2) : "memory"); };
    auto cpf = [&](u32 p0, u32 e) { cpf_go(cpf_off(p0, e)); };
    auto code_again = [&](int n) {
        unsigned long long pc; u32 len, back;
        asm volatile("s_getpc_b64 %0\n1:\n\ts_mov_b32 %1, .Lcode_end-.Lcode_here\n\ts_mov_b32 %2, 1b-.Lcode_here" : "=s"(pc), "=s"(len), "=s"(back));
        const u32 off = min((u32)(((lane & 7) * 4 + (wave8 & 3)) * n + r5) * 128u, len);
        asm volatile("s_mov_b32 m0, %0\n\ts_nop 0\n\tglobal_load_lds_dword %1, %2" :: "s"(lds_addr(smem) + KA_JUNK), "v"(off), "s"(pc - back) : "memory");
    };
    auto w_pre = [&](int k0, int k1) {
        const char* wsrc = (const char*)(w1 + ((xg * 27 + r5) / 6) * 128) - 4096;
#pragma unroll
        for (int kt = k0; kt < k1; ++kt) GLDS4("", lds_addr(smem) + kt * 16384 + 4096 * (wave8 - 4), wsrc + (size_t)kt * (64 * F_ * 2), woff[0], woff[1], woff[2], woff[3]);
    };
    auto offs2 = [&](int part) {
        int ln = lane;
        PIN(ln);
        const int w = wave8 - 4;
        if (part == 0) { w_offs(woff2, w, ln, L_ * 2); asm volatile("" : "+v"(woff2[0]), "+v"(woff2[1]), "+v"(woff2[2]), "+v"(woff2[3])); }
#pragma unroll
        for (int i = 0; i < (part == 1 ? 5 : 0); ++i) {
            const int un = min(64 * i + ln, 287);
            goff[i] = (2 * w + un / 144) * (D_ * 16) + (un % 144) * 16 + 4096 - 1024 * (i & 3);
            PIN(goff[i]);
        }
#pragma unroll
        for (int i = part == 3 ? 4 : 0; i < (part == 3 ? 7 : part == 2 ? 4 : 0); ++i) {
            const int u = min((w * 3 + i - 4) * 64 + ln, 575);
            ro[i] = (i < 4 ? y_msg((xg & 6) + ((ln >> 3) & 1), 54 * (r5 & 3) + min(w * 16 + 4 * i + (ln >> 4), 53)) + (ln & 7) * 16 : (u / 18) * (D_ * 2 - 288) + u * 16 + (u >= 288 ? 48 * D_ * 2 : 0)) + 4096 - 1024 * (i & 3);
            PIN(ro[i]);
        }
    };
    const unsigned long long t_start = __builtin_amdgcn_s_memrealtime();
    {
    const int wave = wave8;
    if (wave < 4) {
        const int ro = (blk * 8 + rr) * D_ + wave * 144;
#pragma unroll
        for (int v = 0; v < 2; ++v) { xv[v] = *(const u32x4*)(x + ro + 72 * v + j8 * 8); x9[v] = x[ro + 72 * v + 64 + j8]; }
        tag = __hip_atomic_load(epg, RLX_AGENT);
    } else if (conv) {
        kp = kparam_load(logA, Aim, Cre, Cim, logdt, Dskip, d0 + ((wave >> 1) & 1), lane);
        kp2 = kparam_load(logA, Aim, Cre, Cim, logdt, Dskip, d0 + 2, lane);
        la1 = __builtin_nontemporal_load((const u32*)(logA + (d0 + 1) * N_ + 2 * lane)); ai1 = __builtin_nontemporal_load((const u32*)(Aim + (d0 + 1) * N_ + 2 * lane));
        dt12 = __builtin_nontemporal_load((const u32_a2*)(logdt + d0 + 1));
        const int ro = (blk * 8 + rr) * D_ + (wave - 4) * 144 + 72;
        xv[0] = *(const u32x4*)(x + ro + j8 * 8); x9[0] = x[ro + 64 + j8];
    }
    volatile LAS u32* cnt = (volatile LAS u32*)(smem + KA_MISC + 256);
    if (tid < 8) cnt[tid] = 0;
    if (tid < 16) cnt[16 + tid] = 0;
    BAR_L0();
    f32x2* chp = (f32x2*)(smem + KA_MISC + 288);
    const int i16 = lane & 15, g = lane >> 4;
    const u32 lb = lds_addr(smem);
    auto biases = [&]() {
    if (wave == 4 || wave == 5)
        asm volatile("s_mov_b32 m0, %0\n\ts_nop 0\n\tglobal_load_lds_dword %1, %2" :: "s"(lb + KB_BIAS + (wave - 4) * 256), "v"(lane * 4),
                     "s"(wave == 4 ? b1 + (conv ? (xg * 27 + r5) / 6 : 0) * 128 : b2 + (((xg & 1) * 32 + r5) >> 2) * 128) : "memory");
    };
    u32 fo1, c1, one1;
    if (wave < 4) {
        __builtin_amdgcn_s_setprio(2);
#pragma unroll
        for (int i = 0; i < 5; ++i) { const int o = lane * 16 + i * 1024; if (o < (wave < 3 ? KCH : 1536)) *(u32x4*)(smem + KA_K + wave * KCH + o) = u32x4{0, 0, 0, 0}; }
        fo1 = (u32)WS_FL + (lane * 256 + blk) * FS; c1 = lb + KA_MISC + 256 + 4; one1 = 1;
        asm volatile("" : "+v"(fo1), "+v"(c1), "+v"(one1) :: "memory");
        int again = 0;
        do {
        asm volatile("" : "+v"(x9[0]), "+v"(x9[1]), "+v"(tag), "+v"(xv[0]), "+v"(xv[1]));
        tag += 1u;
        if (tid == 0) cnt[19] = tag * 0x10001u;
        float a[18];
#pragma unroll
        for (int v = 0; v < 2; ++v) {
#pragma unroll
            for (int i = 0; i < 4; ++i) { a[9 * v + 2 * i] = bf_lo(xv[v][i]); a[9 * v + 2 * i + 1] = bf_hi(xv[v][i]); }
            a[9 * v + 8] = bf_lo(x9[v]);
        }
        PIN(a[0]);
        float mean;
        const float var = row_stats<2>(a, (f32x2*)(smem + KA_MISC), wave, lane, mean, [&]() { lds_arrive(cnt, lane); lds_wait<0>(cnt, 4u); });
        const float m = rbf(mean);
        const float inv = __builtin_amdgcn_rcpf(rbf(__builtin_amdgcn_sqrtf(rbf(rbf(var) + 1e-5f))));
        const float mi = -m * inv;
        const __amdgpu_buffer_rsrc_t rs_u = __builtin_amdgcn_make_buffer_rsrc(ws + WS_U, 0, 256 * UP, 0x00020000);
#pragma unroll
        for (int v = 0; v < 2; ++v) {
            float n[9];
#pragma unroll
            for (int i = 0; i < 9; ++i) n[i] = __builtin_fmaf(a[9 * v + i], inv, mi);
            char* wb = smem + KA_WB + (2 * wave + v) * 1024;
            *(u32x4*)(wb + lane * 16) = u32x4{pk_f16(n[0], n[1]), pk_f16(n[2], n[3]), pk_f16(n[4], n[5]), pk_f16(n[6], n[7])};
            __builtin_amdgcn_raw_buffer_store_b16((u16)pk_f16(n[8], n[8]), rs_u, blk * UP + u_line(wave * 18 + 9 * v + 8) + lane * 2, 0, SC1);
            __builtin_amdgcn_raw_buffer_store_b128(tr8x8(wb, lane), rs_u, blk * UP + u_unit(wave * 144 + 72 * v + lane), 0, SC1);
        }
        if (tid < 24) st128(u32x4{0, 0, 0, 0}, rs_ws, lane * 16, (int)WS_CTL + blk * 384);
        asm volatile("" : "+s"(again));
        } while (__builtin_expect(again, 0));
    } else if (!conv) {
        u32 junk = 0;
        if (wave == 6 && r5 == 27) junk = *(const GAS u32*)(ws + WS_FL + ((xg * NREP * 2 * FS / 8) + lane % (NREP * 2 * FS / 8)) * 128);
        const unsigned long long t0 = __builtin_amdgcn_s_memrealtime();
        while (__builtin_amdgcn_s_memrealtime() - t0 < PFA_DELAY) __builtin_amdgcn_s_sleep(16);
        biases();
        if (wave == 7) junk = *(const GAS u32*)(ws + min((size_t)lane << 19, WS_END));
        asm volatile("" :: "v"(junk));
    } else {
        __builtin_amdgcn_s_setprio(1);
#pragma unroll 1
        for (int it = 0; it < (wave >= 6 ? 2 : 1); ++it) {
            if (it == 0) {
                asm volatile("" : "+v"(dt12), "+v"(la1), "+v"(ai1), "+v"(kp2.la), "+v"(kp2.ai));
                biases();
                h_marks();
                if (wave < 6) {
                    table_t2({kp.la, la1, kp2.la}, {kp.ai, ai1, kp2.ai}, {kp.dt, (u16)dt12, (u16)(dt12 >> 16)}, wave & 1, smem, lane);
                    lds_arrive(cnt + 7, lane);
                }
            }
            const int c = it ? 2 : ((wave >> 1) & 1);
            table_r(kp, c, wave & 1, smem, chp, lane);
            lds_arrive(cnt + 2 + c, lane);
            if (it == 0 && 7 - wave < CP0) cpf(PB1, blk * CP0 + 7 - wave);
            kp = kp2; kp.dt = (u16)(dt12 >> 16);
        }
    }
    __builtin_amdgcn_s_waitcnt(0x0f70);
    {
        u32 old; int olds;
        asm volatile("s_waitcnt vmcnt(0)\n\ts_cmp_gt_u32 %8, 3\n\ts_cbranch_scc1 1f\n\ts_mov_b64 exec, 1\n\tds_add_rtn_u32 %0, %2, %3\n\ts_waitcnt lgkmcnt(0)\n\tv_readfirstlane_b32 %1, %0\n\ts_mov_b64 exec, %7\n\t"
                     "s_cmp_lg_u32 %1, 3\n\ts_cbranch_scc1 1f\n\tglobal_store_dword %4, %5, %6 sc1\n1:\n\ts_mov_b64 exec, -1\n\ts_setprio 0"
                     : "=&v"(old), "=&s"(olds) : "v"(c1), "v"(one1), "v"(fo1), "v"(tag), "s"(ws), "i"((1 << NREP) - 1), "s"(wave) : "memory", "scc");
    }
    if (conv) {
    const int n0 = ((tid >> 2) & 0xf0) + ((tid & 4) << 1) + ((tid >> 3) & 7), gg = (n0 & 1) * 2 + ((tid >> 1) & 1);
    so = KA_RED + (n0 >> 5) * 1024 + (gg * 16 + (((n0 & 31) >> 1) ^ (2 * (gg & 1) + 8 * (gg >> 1)))) * 16 + (tid & 1) * 8;
    yo = y_msg(n0, xg * 27 + r5) + (tid & 3) * 16;
#pragma unroll
    for (int k = 0; k < 4; ++k) yi[k] = (u32)WS_Y + y_off(2 * (wave & 3) + (wave >> 2), k);
    asm volatile("" : "+v"(so), "+v"(yo), "+v"(yi[0]), "+v"(yi[1]), "+v"(yi[2]), "+v"(yi[3]));
    if (wave < 3) {
        const u32 kb = lb + KA_K + wave * KCH - 8;
        const u32 ta = lb + KA_TAB + wave * TBL + i16 * 64 + ((g ^ ((-(i16 >> 2)) & 3)) << 4);
        const int r1 = min(2 * (i16 >> 2) + (i16 & 3), 8);
        const u32 t1 = lb + KA_TAB + wave * TBL + (48 + r1) * 64 + ((g ^ ((-(r1 >> 2)) & 3)) << 4);
        u32x4 T[8], A0[8], A1[8], AX[8];
        f32x4 a0, a1, ax;
        lds_wait<1>(cnt + 7, 2u);
#pragma unroll
        for (int ks = 0; ks < 8; ++ks) DS_R128(T[ks], ta, ks * TKS);
        WAIT_L0();
        lds_wait<0>(cnt + 2 + wave, 2u);
        __builtin_amdgcn_s_setprio(2);
#pragma unroll
        for (int ks = 0; ks < 4; ++ks) { DS_R128(A0[ks], ta, ks * TKS + 1024); DS_R128(A1[ks], t1, ks * TKS); DS_R128(AX[ks], ta, ks * TKS + 2048); }
        float dsr;
        asm volatile("ds_read_b32 %0, %1" : "=v"(dsr) : "v"(lb + KA_MISC + 292 + wave * 8));
        WAIT_L0();
#pragma unroll
        for (int ks = 4; ks < 8; ++ks) { DS_R128(A0[ks], ta, ks * TKS + 1024); DS_R128(A1[ks], t1, ks * TKS); DS_R128(AX[ks], ta, ks * TKS + 2048); }
        MFMA_F16_Z(a0, A0[0], T[0]); MFMA_F16_Z(a1, A1[0], T[0]); MFMA_F16_Z(ax, T[0], AX[0]);
#pragma unroll
        for (int ks = 1; ks < 8; ++ks) {
            if (ks == 4) WAIT_L0();
            MFMA_F16(a0, A0[ks], T[ks]); MFMA_F16(a1, A1[ks], T[ks]); MFMA_F16(ax, T[ks], AX[ks]);
        }
        asm volatile("s_nop 7\n\ts_nop 3" : "+v"(a0), "+v"(a1), "+v"(ax), "+v"(dsr));
        if (lane == 0) a0[0] += dsr;
        const u32 pz = kb + (542 - 16 * i16 - 4 * g) * 2;
        put4<1216>(pz, a0[0], a0[1], a0[2], a0[3]);
        put4<1216>(pz - 512, a1[0], a1[0], a1[1], a1[2]);
        if (lane == 63) put4<1216>(kb + (542 - 512) * 2, a1[2], a1[2], a1[2], 0.f);
        const int ph = i16 < 8 ? (i16 & 3) : 4 + (i16 & 1), a = i16 < 8 ? (i16 >> 2) : ((i16 - 8) >> 1);
        put4<192>(kb + 2432 + ph * 384 + (62 - 16 * a - 4 * g) * 2, ax[0], ax[1], ax[2], ax[3]);
        lds_arrive(cnt + 5, lane);
    } else if (wave == 3) {
        const __amdgpu_buffer_rsrc_t rs_f = __builtin_amdgcn_make_buffer_rsrc(ws + WS_FL + (blk % NREP) * 256 * FS, 0, 256 * FS, 0x00020000);
        u32 go[3];
#pragma unroll
        for (int i = 0; i < 3; ++i) { const int e = 64 * i + lane; go[i] = (e / 3) * UP + (e % 3) * 16 + 4096 - 1024 * i; PIN(go[i]); }
        const char* src = ws + WS_U + u_unit(d0) - 4096;
        asm volatile("" : "+s"(src));
        const unsigned long long t0 = __builtin_amdgcn_s_memrealtime();
        __builtin_amdgcn_s_sleep(POLL1_FIRST);
        for (;;) {
            u32x4 f;
            for (int i = 0; i < 4; ++i) f[i] = __builtin_amdgcn_raw_buffer_load_b32(rs_f, lane * FS, i * 64 * FS, SC1);
            if (__all(f[0] == tag && f[1] == tag && f[2] == tag && f[3] == tag)) break;
            if (__builtin_amdgcn_s_memrealtime() - t0 > SPIN_LIMIT) break;
            __builtin_amdgcn_s_sleep(POLL1_SLEEP);
        }
#pragma unroll
        for (int i = 0; i < 4; ++i) GLDS3(" sc1", lb + KA_U + 1536 + i * 3072, src + (size_t)i * (64 * UP), go[0], go[1], go[2]);
        if (lane == 0) cnt[6] = 1;
        __builtin_amdgcn_raw_buffer_store_b64(u32x2{tag, (u32)__builtin_amdgcn_s_memrealtime()}, rs_ws, (r5 | lane) == 0 ? (int)WS_EP + (HINT_T + xg * 32) * 4 : 0x40000000, 0, SC1);
        __builtin_amdgcn_raw_buffer_store_b32(tag, rs_ws, (blk | lane) == 0 ? (int)WS_EP : 0x40000000, 0, SC1);
        lds_wait<0>(cnt + 5, 3u);
    }
    const u32 ra = lb + KA_RED + (g * 16 + (i16 ^ (2 * (g & 1) + 8 * (g >> 1)))) * 16;
    if (wave < 4) {
        f32x4 acc[8];
        const u32 ka = lb + KA_K + (i16 & 1) * 1216 + (542 - 16 - (i16 & 14) + 8 * g) * 2 - 1024;
        const u32 ub = lb + KA_U + (30 + 2 * i16 + g) * 48;
        u32 ca = lb + KA_MISC + 256 + 16 * 4, one = 1;
        asm volatile("" : "+v"(ca), "+v"(one));
        if (wave < 3) {
            Frag A[8][2], A16, T2f[2][3], T3f[4];
            u32x4 A32;
            const u32 kc = ka + wave * KCH;
            const u32 tc = lb + KA_K + wave * KCH + 2432 + (g >> 1) * 384 + (i16 & 1) * 192 + (62 - (i16 & 14) + 8 * (g & 1)) * 2 - 96, tc3 = tc + 1536;
            frag_pair<0>(A[0], kc); frag_pair<1>(A[1], kc); frag_pair<2>(A[2], kc); frag_pair<3>(A[3], kc);
            { frag_pair<4>(A[4], kc); frag_pair<5>(A[5], kc); frag_pair<6>(A[6], kc); frag_pair<7>(A[7], kc); }
            {
                const u32 k1024 = *(const u16*)(smem + KA_K + wave * KCH + 2432 + 3 * 384 + 31 * 2);
#pragma unroll
                for (int w = 0; w < 4; ++w) {
                    const int e = 8 * g + 2 * w - i16;
                    A32[w] = ((e == 12 || e == 13) ? k1024 : 0u) | ((e == 11 || e == 12) ? (k1024 << 16) : 0u);
                }
            }
            const u32 ba = ub + 16 * wave - 192 * 7;
            const int q = i16 >> 2, p = lane & 3;
            const u32 sa2 = lb + KA_SIG + wave * SCH + (g >> 1) * 3088 + (128 * (g & 1) + 16 * q + 4 * p) * 2;
            const u32 sa3 = lb + KA_SIG + wave * SCH + 6176 + (g >> 1) * 2048 + (128 * (g & 1) + 16 * q + 4 * p) * 2;
            const u32 rb = ra + 24576 + wave * 8192;
            u32x4 U[8][8];
            u32x2 h[8][2], h2[8][2];
            auto rd = [&](int jd, int m) { ds_r128(U[jd][m], ba, 192 * (7 - jd) + 1536 * m); };
            PIN(A32);
            __builtin_amdgcn_s_setprio(3);
            BAR_L0();
#pragma unroll
            for (int k = 0; k < 16; ++k) rd(k >> 1, k & 1);
            WAIT_L0();
            auto gb = [&](int k) { if (k < 6) rd(0, 2 + k); if (k == 6) frag_ld<0>(A16, kc); };
            cv_part<0, 2, true, 0>(acc, A[0], U[0], gb); cv_part<0, 2, false, 3>(acc, A[1], U[1], gb);
            cv_part<0, 2, false, 6>(acc, A[2], U[2], gb); cv_part<0, 2, false, 9>(acc, A[3], U[3], gb);
            cv_part<0, 2, false, 9>(acc, A[4], U[4], gb); cv_part<0, 2, false, 9>(acc, A[5], U[5], gb);
            cv_part<0, 2, false, 9>(acc, A[6], U[6], gb); cv_part<0, 2, false, 9>(acc, A[7], U[7], gb);
            WAIT_L0();
            cv_part<2, 8, true, 0>(acc, A[0], U[0], [&](int k) {
                if (k == 1) { asm volatile("s_nop 2"); ds_w128(rb, acc[0], 0); asm volatile("s_nop 0"); ds_w128(rb, acc[1], 1024); lds_bump(ca, one); __builtin_amdgcn_s_setprio(0); }
                if (k >= 1 && k < 7) rd(1, 1 + k); });
            const u32x4 A16v = A16.v();
#pragma unroll
            for (int m = 0; m < 6; ++m) MFMA_F16(acc[m + 2], A16v, U[0][m]);
#pragma unroll
            for (int m = 0; m < 4; ++m) MFMA_F16(acc[m + 4], A32, U[0][m]);
            WAIT_L0();
            cv_part<2, 8, false, 0>(acc, A[1], U[1], [&](int k) { if (k < 6) rd(2, 2 + k);
                if (k == 0) frag_ld<24>(T2f[0][0], tc); if (k == 1) frag_ld<16>(T2f[0][1], tc); if (k == 2) frag_ld<8>(T2f[0][2], tc); });
            WAIT_L0();
            cv_part<2, 8, false, 0>(acc, A[2], U[2], [&](int k) { if (k < 6) rd(3, 2 + k);
                if (k == 0) frag_ld<216>(T2f[1][0], tc); if (k == 1) frag_ld<208>(T2f[1][1], tc); if (k == 2) frag_ld<200>(T2f[1][2], tc); });
            WAIT_L0();
            cv_part<2, 8, false, 0>(acc, A[3], U[3], [&](int k) { if (k < 6) rd(4, 2 + k);
                if (k == 0) frag_ld<24>(T3f[0], tc3); if (k == 1) frag_ld<16>(T3f[1], tc3); if (k == 2) frag_ld<8>(T3f[2], tc3); if (k == 3) frag_ld<0>(T3f[3], tc3); });
            WAIT_L0();
            lds_wait<1>(cnt + 20, 4u);
            cv_part<2, 8, false, 0>(acc, A[4], U[4], [&](int k) {
                if (k < 8) { DS_TR(h[k][0], sa2, 512 * (k >> 1) + 16 * (1 - (k & 1))); DS_TR(h[k][1], sa2, 512 * (k >> 1) + 128 + 16 * (1 - (k & 1))); } });
            WAIT_L0();
            const u32x4 T2[2][3] = {{T2f[0][0].v(), T2f[0][1].v(), T2f[0][2].v()}, {T2f[1][0].v(), T2f[1][1].v(), T2f[1][2].v()}};
            if (wave == 0) lds_bump(ca + 16, one);
#pragma unroll
            for (int k = 0; k < 24; ++k) {
                const int b = k >> 3, pr = (k >> 2) & 1, be = k & 3, f = 2 * be + pr;
                MFMA_F16(acc[2 + be + b], (u32x4{h[f][0][0], h[f][0][1], h[f][1][0], h[f][1][1]}), T2[pr][b]);
                if (k < 4) { DS_TR(h2[k][0], sa2, 512 * (4 + (k >> 1)) + 16 * (1 - (k & 1))); DS_TR(h2[k][1], sa2, 512 * (4 + (k >> 1)) + 128 + 16 * (1 - (k & 1))); }
                else if (k < 8) { DS_TR(h2[k][0], sa3, 512 * (k - 4)); DS_TR(h2[k][1], sa3, 512 * (k - 4) + 128); }
                if (k == 12) DS_W128(rb, acc[2], 2048);
                if (k == 20) DS_W128(rb, acc[3], 3072);
            }
            WAIT_L0();
            const u32x4 T3[4] = {T3f[0].v(), T3f[1].v(), T3f[2].v(), T3f[3].v()};
            u32x4 X[8];
#pragma unroll
            for (int k = 0; k < 8; ++k) X[k] = u32x4{h2[k][0][0], h2[k][0][1], h2[k][1][0], h2[k][1][1]};
            MFMA_F16(acc[6], X[0], T2[0][0]); MFMA_F16(acc[7], X[2], T2[0][0]);
            MFMA_F16(acc[4], X[4], T3[0]); MFMA_F16(acc[5], X[5], T3[0]);
            MFMA_F16(acc[6], X[1], T2[1][0]); MFMA_F16(acc[7], X[3], T2[1][0]);
            MFMA_F16(acc[5], X[4], T3[1]);
            MFMA_F16(acc[6], X[6], T3[0]); MFMA_F16(acc[7], X[0], T2[0][1]);
            MFMA_F16(acc[6], X[5], T3[1]); MFMA_F16(acc[7], X[1], T2[1][1]);
            MFMA_F16(acc[6], X[4], T3[2]); MFMA_F16(acc[7], X[7], T3[0]);
            DS_W128(rb, acc[4], 4096);
            MFMA_F16(acc[7], X[6], T3[1]); MFMA_F16(acc[7], X[5], T3[2]);
            DS_W128(rb, acc[5], 5120);
            MFMA_F16(acc[7], X[4], T3[3]);
            asm volatile("s_nop 1");
            DS_W128(rb, acc[6], 6144);
            asm volatile("s_nop 4");
            DS_W128(rb, acc[7], 7168);
            y_prep();
        } else {
            Frag A[9][2];
            u32x4 U[9][8];
            f32x4 acc2[8];
            const u32 kc[3] = {ka, ka + KCH, ka + 2 * KCH};
            const u32 ba = ub - 192 * 7;
            auto rd = [&](int un, int m) { ds_r128(U[un][m], ba, 192 * (2 - un % 3) + 16 * (un / 3) + 1536 * m); };
            y_prep();
            frag_pair<5>(A[0], kc[0]);
            WAIT_V(2);
            BAR_L0();
#pragma unroll
            for (int m = 1; m < 8; ++m) rd(0, m);
            WAIT_L0();
            auto unit = [&](auto un) { constexpr int H = decltype(un)::v, C = H / 3;
                cv_part<2, 8, H % 3 == 0, 0>(C & 1 ? acc2 : acc, A[H], U[H], [&](int k) {
                    if constexpr (H < 8) { if (k < 7) rd(H + 1, k + 1); if (k == 0) frag_pair<5 + (H + 1) % 3>(A[H + 1], kc[(H + 1) / 3]); }
                    if (H % 3 == 0 && H > 0 && k >= 6) ds_w128(ra, (C & 1 ? acc : acc2)[k - 4], ((C - 1) * 8 + k - 4) * 1024);
                });
                if (H < 8) WAIT_L0();
                if (H == PUBU) {
                    lds_wait<0>(cnt + 16, 3u);
                    publish(IC<-3>{}, IC<1>{}, IC<1>{});
                }
            };
            unit(IC<0>{}); unit(IC<1>{}); unit(IC<2>{}); unit(IC<3>{}); unit(IC<4>{}); unit(IC<5>{}); unit(IC<6>{}); unit(IC<7>{}); unit(IC<8>{});
            asm volatile("s_nop 2");
#pragma unroll
            for (int t = 2; t < 8; ++t) DS_W128(ra, acc[t], (16 + t) * 1024);
        }
    } else {
        auto offs1 = [&]() {
            int ln = lane;
            PIN(ln);
            w_offs(woff, wave - 4, ln, F_ * 2);
#pragma unroll
            for (int i = 0; i < 3; ++i) {
                const int un = 192 * (wave - 4) + 64 * i + ln;
                hoff[i] = (un / 96) * (D_ * 16) + (un % 96) * 16 + 4096 - 1024 * i;
            }
            asm volatile("" : "+v"(woff[0]), "+v"(woff[1]), "+v"(woff[2]), "+v"(woff[3]), "+v"(hoff[0]), "+v"(hoff[1]), "+v"(hoff[2]));
        };
        offs1();
        u32 cp = cpf_off(0, blk * CP1 + wave - 4);
        PIN(cp);
        lds_wait<1>(cnt + 6, 1u);
        if (wave - 4 < CP1) cpf_go(cp);
        BAR_L0();
        __builtin_amdgcn_s_setprio(2);
        auto pool = [&](int c, int n, bool far) {
            const char* up = smem + KA_U + (29 + n) * 48 + 16 * c;
            u32 w[16], od[14], t2[14], v4[13];
#pragma unroll
            for (int k = 0; k < 4; ++k) { const u32x4 v = *(const u32x4*)(up + 48 * k); w[4 * k] = v[0]; w[4 * k + 1] = v[1]; w[4 * k + 2] = v[2]; w[4 * k + 3] = v[3]; }
#pragma unroll
            for (int k = 2; k < 14; ++k) od[k] = __builtin_amdgcn_alignbit(w[k + 1], w[k], 16);
#pragma unroll
            for (int k = 3; k < 14; ++k) t2[k] = pk_add_h(w[k], od[k - 1]);
#pragma unroll
            for (int k = 4; k < 13; ++k) v4[k] = pk_add_h(t2[k], t2[k - 1]);
            u32x4 s1, s2, so, se;
#pragma unroll
            for (int o = 0; o < 4; ++o) {
                s1[o] = pk_add_h(od[10 + o], t2[10 + o]);
                s2[o] = pk_add_h(v4[9 + o], w[7 + o]);
                so[o] = pk_add_h(pk_add_h(v4[9 + o], t2[7 + o]), od[9 + o]);
                se[o] = pk_add_h(pk_add_h(v4[6 + o], v4[4 + o]), w[2 + o]);
            }
            char* sp = smem + KA_SIG + c * SCH;
            *(u32x4*)(sp + 16 + n * 16) = s1;
            *(u32x4*)(sp + 3088 + 16 + n * 16) = s2;
            if (far) { *(u32x4*)(sp + 6176 + n * 16) = so; *(u32x4*)(sp + 8224 + n * 16) = se; }
        };
        if (wave < 7) { pool(wave - 4, lane, true); pool(wave - 4, 64 + lane, true); }
        else {
            pool(0, 128 + lane, false); pool(1, 128 + lane, false); pool(2, 128 + lane, false);
            if (lane < 6) *(u32x4*)(smem + KA_SIG + (lane >> 1) * SCH + (lane & 1) * 3088) = u32x4{0, 0, 0, 0};
        }
        lds_arrive(cnt + 20, lane);
        __builtin_amdgcn_s_setprio(0);
        code_again(27);
        __builtin_amdgcn_s_sleep(GM_DELAY);
        g_marks();
        if (CP2 && !early && 7 - wave < CP2) cpf(PB0, (blk - 64) * CP2 + 7 - wave);
        y_prep();
        if (early) {
            lds_wait<1>(cnt + 20, 5u);
            __builtin_amdgcn_s_sleep(YE_DELAY);
            Y_LOAD("", "", blk);
        }
    }
    BAR_L0();
    Y_LOAD("s_cmp_lt_u32 %9, 64\n\ts_cbranch_scc0 1f\n\t", "\n1:", esel);
    publish(IC<0>{}, IC<9>{}, IC<8>{}, wave < 4 ? 0x40000000 : (int)WS_Y);
    Y_WAIT("s_cmp_lt_u32 %5, 64\n\ts_cbranch_scc0 1f\n\t", 2, "\n1:");
    if (wave >= 4 && !early) w_pre(0, 2);
    }
    }

    u16* hh8 = (u16*)(ws + WS_HH); u16* G8 = (u16*)(ws + WS_G);
    char* slabs = ws + WS_SLAB; u32* ctl = (u32*)(ws + WS_CTL);
    const int wave = wave8 & 3;
    const u32 lbase = lds_addr(smem);
    volatile LAS u32* cnt = (volatile LAS u32*)(smem + KA_MISC + 256);
    const int g = lane >> 4, q = (lane & 15) >> 2, p = lane & 3, i16 = lane & 15;
    GAS u32* ctlg = (GAS u32*)ctl;
#define E3_NEED ((lane & 7) < 6 ? 36u : 20u)
    auto late = [&]() { return __builtin_amdgcn_s_memrealtime() - t_start > SPIN_LIMIT; };
    auto y_get = [&](int role) {
        u32x2 (&y)[4] = y1[0];
        const u32 want = (rr & 1) ? ytag >> 16 : ytag & 0xffffu;
        if (role != 2) Y_WAIT("", 0, "");
        for (bool have = true;; have = false) {
            if (!have) Y_LOAD("", "\n\ts_waitcnt vmcnt(0)", blk);
            const u32 bad = ((y[0][1] >> 16) ^ want) | ((y[1][1] >> 16) ^ want) | ((y[2][1] >> 16) ^ want) | ((y[3][1] >> 16) ^ want);
            if (!__any(bad != 0) || late()) break;
        }
    };
    auto ln2 = [&](auto role, int h0, int slot, auto synced) {
        constexpr int ROLE = decltype(role)::v, NH = ROLE ? 1 : 2, NW = 8 / NH;
        const int j = j8;
        const int uo = (blk * D_ + h0 * 72) * 8;
        const int pos9 = j - min(3 * sub9, 5);
        u32x2 (&yg)[2][4] = y1;
        int again = 0;
        do {
        if (NH == 1) y_get(ROLE);
        else for (;;) {
            const u32 yt = tag * 0x10001u, want = (rr & 1) ? yt >> 16 : yt & 0xffffu;
            u32 bad = 0;
#pragma unroll
            for (int v = 0; v < NH; ++v)
#pragma unroll
                for (int k = 0; k < 4; ++k) yg[v][k] = __builtin_amdgcn_raw_buffer_load_b64(rs_ws, y_off(h0 + v, k), (int)WS_Y, SC1);
#pragma unroll
            for (int v = 0; v < NH; ++v)
#pragma unroll
                for (int k = 0; k < 4; ++k) bad |= (yg[v][k][1] >> 16) ^ want;
            if (!__any(bad != 0) || late()) break;
            __builtin_amdgcn_s_sleep(16);
        }
        if (ROLE == 1) w_pre(2, 4);
        char* wb = smem + KB_WB + h0 * 1024;
        float a[9 * NH];
#pragma unroll
        for (int v = 0; v < NH; ++v) {
            const float y[8] = {bf_lo(yg[v][0][0]), bf_hi(yg[v][0][0]), bf_lo(yg[v][0][1]), bf_lo(yg[v][1][0]), bf_hi(yg[v][1][0]), bf_lo(yg[v][1][1]), bf_hi(yg[v][2][0]), bf_lo(yg[v][2][1])};
            u32x4 hv;
#pragma unroll
            for (int i = 0; i < 4; ++i) {
                hv[i] = pk_bf16(bf_lo(xv[v][i]) + y[2 * i], bf_hi(xv[v][i]) + y[2 * i + 1]);
                a[9 * v + 2 * i] = bf_lo(hv[i]); a[9 * v + 2 * i + 1] = bf_hi(hv[i]);
            }
            a[9 * v + 8] = rbf(bf_lo(x9[v]) + (pos9 == 0 ? bf_lo(yg[v][3][0]) : pos9 == 1 ? bf_hi(yg[v][3][0]) : bf_lo(yg[v][3][1])));
        }
        float mean;
        const float var = row_stats<NH>(a, (f32x2*)(smem + KB_RED), slot, lane, mean, [&]() { lds_arrive(cnt + 17, lane); lds_wait<0>(cnt + 17, NW); synced(); });
        const int hb = (int)WS_HH;
        const float r1 = __builtin_amdgcn_rsqf(var + 1e-5f);
        const float inv = r1 * __builtin_amdgcn_rsqf(var * r1 * r1 + 1e-5f);
        const float mi = -mean * inv;
#pragma unroll
        for (int v = 0; v < NH; ++v) {
            float n[9];
#pragma unroll
            for (int i = 0; i < 9; ++i) n[i] = __builtin_fmaf(a[9 * v + i], inv, mi);
            *(u32x4*)(wb + v * 1024 + lane * 16) = u32x4{pk_bf16(n[0], n[1]), pk_bf16(n[2], n[3]), pk_bf16(n[4], n[5]), pk_bf16(n[6], n[7])};
            __builtin_amdgcn_raw_buffer_store_b32(clean(pk_bf16(n[8], dpp_get<0xB1>(n[8]))), rs_ws, (lane & 1) ? 0x40000000 : (uo + 576 * v + 512 + lane) * 2, hb, SC1);
            st128(clean4(tr8x8(wb + v * 1024, lane)), rs_ws, (uo + 576 * v + lane * 8) * 2, hb);
        }
        if (ROLE == 3) asm volatile("" : "+s"(again));
        } while (__builtin_expect(again, 0));
        if (ROLE == 0) {
            WAIT_V(0);
            lds_arrive(cnt + 18, lane);
            if (wave8 == 0) {
                lds_wait<0>(cnt + 18, NW);
                if (lane < E3R) __hip_atomic_fetch_add(ctlg + CTL_E3 + (lane * 8 + 6 + (blk & 1)) * 32, 1u, RLX_AGENT);
            }
        }
    };
    const int kq = xg >> 1, tile = (xg & 1) * 32 + r5;
    const int s0 = (tile >> 2) * 128, d2 = (tile & 3) * 144;
    const bool g1 = r5 < 27;
    const int id = xg * 27 + r5;
    const int fg = id / 6, mt1 = id % 6;
    const int f0 = fg * 128, d1 = mt1 * 96;
    u32x2 hv[3];
    auto residual = [&]() {
        u32x2 yp[3], yq[3];
#pragma unroll
        for (int e = 0; e < 3; ++e) {
            const int m = e < 2 ? (wave8 & 6) + e : 8, r = i16 + 16 * (wave8 & 1);
            const char* yr = smem + KB_RES + ((2 * m + (g >> 1)) * 3 + (g & 1)) * 256 + (i16 >> 3) * 128 + (wave8 & 1) * 64 + (i16 & 7) * 8;
            yp[e] = *(const u32x2*)yr; yq[e] = *(const u32x2*)(yr + 256); hv[e] = *(const u32x2*)(smem + KB_RX + r * 288 + 32 * m + 8 * g);
        }
#pragma unroll
        for (int e = 0; e < 3; ++e) {
            const u32 w0 = __builtin_amdgcn_perm(yp[e][1], yp[e][0], (g & 1) ? 0x05040302u : 0x03020100u);
            const u32 w1 = __builtin_amdgcn_perm(yq[e][0], (g & 1) ? yq[e][1] : yp[e][1], (g & 1) ? 0x01000706u : 0x05040100u);
            hv[e] = u32x2{pk_bf16(bf_lo(hv[e][0]) + bf_lo(w0), bf_hi(hv[e][0]) + bf_hi(w0)), pk_bf16(bf_lo(hv[e][1]) + bf_lo(w1), bf_hi(hv[e][1]) + bf_hi(w1))};
        }
        asm volatile("" : "+v"(hv[0]), "+v"(hv[1]), "+v"(hv[2]) :: "memory");
    };
    if (early) { ln2(IC<2>{}, 2 * wave + (wave8 >> 2), wave8, [] {}); if (wave8 >= 4) w_pre(0, 2); }
    if (!conv && wave8 < 4) {
        if (wave8 == 0) {
            unsigned long long v;
            do { __builtin_amdgcn_s_sleep(16); v = __hip_atomic_load((GAS unsigned long long*)(epg + HINT_T + xg * 32), RLX_AGENT); } while ((u32)v != tag && !late());
            cnt[6] = (u32)(v >> 32) | 1u;
        }
        lds_wait<8>(cnt + 6, 1u);
        while ((int)((u32)__builtin_amdgcn_s_memrealtime() - cnt[6]) < SPARE_Y && !late()) __builtin_amdgcn_s_sleep(16);
        ln2(IC<0>{}, 2 * wave, wave, [] {});
        if (wave8 == 0) {
            unsigned long long v;
            do { __builtin_amdgcn_s_sleep(6); v = __hip_atomic_load((GAS unsigned long long*)(epg + HINT_G + xg * 32), RLX_AGENT); } while ((u32)v != tag * 0x10001u && !late());
            cnt[16] = ((u32)(v >> 32) + STOP_DT) | 1u;
        }
        if (wave8 == 1)
            while (!__all(__hip_atomic_load(ctlg + CTL_E3 + ((blk & (E3R - 1)) * 8 + (lane & 7)) * 32, RLX_AGENT) >= E3_NEED) && !late()) __builtin_amdgcn_s_sleep(16);
    }
    if (!conv) __builtin_amdgcn_s_waitcnt(0x0f70);
    if (wave8 >= 4) {
        const int back = xg & 1;
        const long wstep = (back ? -128 : 128) * (L_ * 2), gstep = (back ? -8 : 8) * (D_ * 16);
        const char* w2src = (const char*)(w2 + s0) + (size_t)(kq * 1152 + back * 1024 + (wave >> 1) * 32) * (L_ * 2) - 4096;
        u32 lw = lbase + 4096 * wave;
        asm volatile("" : "+s"(lw));
        auto issue_w = [&](const char* ws, int slot) { GLDS4("", lw + slot * 16384, ws, woff[0], woff[1], woff[2], woff[3]); };
        const u32 pl = lbase + KA_MISC + 384;
        const u32 e3o = (u32)(CTL_E3 + ((blk & (E3R - 1)) * 8 + (lane & 7)) * 32) * 4;
        const u32 e4o = (u32)(CTL_E4 + (((kq * 4 + (tile & 3)) * 4 + ((tile >> 2) & 3)) * 4 + (lane & 3)) * 32) * 4;
        auto poll_start = [&](u32 o) { if (lane < 8) asm volatile("s_mov_b32 m0, %0\n\ts_nop 0\n\tglobal_load_lds_dword %1, %2 sc1" :: "s"(pl), "v"(o), "s"(ctl) : "memory"); };
        auto poll_get = [&]() { u32 v; asm volatile("ds_read_b32 %0, %1\n\ts_waitcnt lgkmcnt(0)" : "=v"(v) : "v"(pl + (lane & 7) * 4) : "memory"); return v; };
        auto poll_now = [&](u32 o) { u32 v; asm volatile("global_load_dword %0, %1, %2 sc1\n\ts_waitcnt vmcnt(0)" : "=v"(v) : "v"(o), "s"(ctl) : "memory"); return v; };
        auto e3_wait = [&](u32 v, int shards) {
            while (!__all(v >= E3_NEED || (lane & 7) >= shards) && !late()) { __builtin_amdgcn_s_sleep(1); v = poll_now(e3o); }
        };
        auto e4_wait = [&](int v, int need) {
            for (;;) {
                v += __builtin_amdgcn_update_dpp(0, v, 0xB1, 0xf, 0xf, true);
                v += __builtin_amdgcn_update_dpp(0, v, 0x4E, 0xf, 0xf, true);
                if (__builtin_amdgcn_readfirstlane(v) >= need || late()) break;
                __builtin_amdgcn_s_sleep(2);
                v = (int)poll_now(e4o);
            }
        };
        offs2(0); offs2(1); offs2(2); offs2(3);
        auto issue_w2 = [&](const char* ws, int slot) { GLDS4("", lw + slot * 16384, ws, woff2[0], woff2[1], woff2[2], woff2[3]); };
        if (g1) {
            if (!early) __builtin_amdgcn_s_sleep(YL_DELAY);
            Y_LOAD("s_cmp_lt_u32 %9, 64\n\ts_cbranch_scc1 1f\n\t", "\n1:", blk);
            const char* wsrc = (const char*)(w1 + f0) - 4096;
            const char* hsrc = (const char*)(hh8 + (size_t)d1 * 8) - 4096;
            auto issue_h = [&](int kt) { GLDS3(" sc1", lbase + 65536 + (kt & 3) * 12288 + 3072 * wave, hsrc + (size_t)kt * (8 * D_ * 16), hoff[0], hoff[1], hoff[2]); };
            auto issue = [&](int kt) { issue_w(wsrc + (size_t)kt * (64 * F_ * 2), kt & 3); issue_h(kt); };
            auto check_h = [&](int kt, auto more) {
                const u32 a = lbase + 65536 + (kt & 3) * 12288 + 3072 * wave + lane * 16;
                u32x4 v0, v1, v2;
                DS_R128(v0, a, 0); DS_R128(v1, a, 1024); DS_R128(v2, a, 2048);
                more();
                for (;;) {
                    asm volatile("s_waitcnt lgkmcnt(0)" : "+v"(v0), "+v"(v1), "+v"(v2) :: "memory");
                    if (!__any(max5(max5(max5(0, v0), v1), v2) == ~0u) || late()) break;
                    if (H_RETRY) __builtin_amdgcn_s_sleep(H_RETRY);
                    issue_h(kt); WAIT_V(0);
                    DS_R128(v0, a, 0); DS_R128(v1, a, 1024); DS_R128(v2, a, 2048);
                }
            };
            int pass = 0;
#pragma clang loop unroll(disable)
            for (;; pass = 1) {
            asm volatile("" : "+s"(pass), "+s"(wsrc), "+s"(hsrc), "+s"(w2src));
            if (!pass && !early) ln2(IC<1>{}, 2 * wave + 1, wave8, [&] { issue_h(0); issue_h(1); });
            const bool asked = !early && !pass;
            if (!asked) {
                if (pass) WAIT_V(0);
                issue_h(0); issue_h(1);
#pragma unroll
                for (int kt = 2; kt < 4; ++kt) issue_w(wsrc + (size_t)kt * (64 * F_ * 2), kt);
                WAIT_V(11);
            } else WAIT_V(5);
            check_h(0, [] {}); BAR();
            issue_h(2);
            if (asked) WAIT_V(5); else WAIT_V(11);
            check_h(1, [&] { issue_h(3); }); BAR();
            issue(4); WAIT_V(10);
            check_h(2, [] {});
            BAR();
            const char* wp = wsrc + (size_t)5 * (64 * F_ * 2); const char* hp = hsrc + (size_t)5 * (8 * D_ * 16);
            const u32 lh = lbase + 65536 + 3072 * wave;
#pragma unroll
            for (int kt = 3; kt < 30; ++kt) {
                GLDS4("", lw + ((kt + 2) & 3) * 16384, wp, woff[0], woff[1], woff[2], woff[3]);
                GLDS3(" sc1", lh + ((kt + 2) & 3) * 12288, hp, hoff[0], hoff[1], hoff[2]);
                wp += 64 * F_ * 2; hp += 8 * D_ * 16;
                asm volatile("" : "+s"(wp), "+s"(hp));
                if (kt == NV - 5 && wave8 == 4) poll_start(e3o);
                if (kt == HINT_KT && wave8 == 5 && r5 == 0 && lane == 0)
                    asm volatile("global_store_dwordx2 %0, %1, %2 sc1" :: "v"((u32)(WS_EP - WS_CTL) + (HINT_G + xg * 32) * 4), "v"(u32x2{cnt[19], (u32)__builtin_amdgcn_s_memrealtime()}), "s"(ctl) : "memory");
                WAIT_V(14);
                if (kt == 3) check_h(kt, [] {});
                if (kt == NV - 3 && wave8 == 4) e3_wait(poll_get(), 8);
                BAR();
            }
            issue_w2(w2src, 0);
            code_again(27);
            WAIT_V(12);
            BAR(); issue_w2(w2src + wstep, 1);
            WAIT_V(9);
            BAR(); issue_w2(w2src + 2 * wstep, 2);
            BAR(); issue_w2(w2src + 3 * wstep, 3);
            if (__builtin_expect(pass || !__builtin_amdgcn_readfirstlane(cnt[22]), 1)) break;
            WAIT_V(0);
#pragma unroll
            for (int kt = 0; kt < 2; ++kt) issue_w(wsrc + (size_t)kt * (64 * F_ * 2), kt);
            }
        } else {
#pragma unroll
            for (int kt = 0; kt < 4; ++kt) issue_w2(w2src + kt * wstep, kt);
            const u32 t = (r5 - 27) * 256 + (threadIdx.x - 256), pw = ((r5 - 27) * 8 + xg) * 4 + wave;
            const u32 o1 = (t / 10) * (F_ * 2) + (((27 * xg) / 6) * 2 + t % 10) * 128;
            constexpr int N1 = 16, R1 = 128;
            int i1 = 0, i2 = 0;
            constexpr int P2 = PB0 + 76 * CP2, N2 = (1152 - P2 + 79) / 80;
            while (i1 < N1 || i2 < N2) {
                const u32 ts = __builtin_amdgcn_readfirstlane(cnt[16]);
                if ((ts && (int)((u32)__builtin_amdgcn_s_memrealtime() - ts) >= 0) || late()) break;
                const u32 te = __builtin_amdgcn_readfirstlane(cnt[6]);
                const int now = te ? (int)((u32)__builtin_amdgcn_s_memrealtime() - te) : min((int)((u32)__builtin_amdgcn_s_memrealtime() - (u32)t_start) - 390, 0);
                if (i1 < N1 && now >= PACE_A + PACE_B * i1) { PREFETCH("", lbase + KA_JUNK, w1, o1 + i1 * (R1 * F_ * 2)); ++i1; }
                else if (i2 < N2) {
                    constexpr u32 NQ = 4 * (1152 - P2);
                    const u32 a = min((i2 * 160 + pw) * 64 + lane, NQ * 32 - 1), b = a - NQ * 8, k = b % 24;
                    const u32 q = a < NQ * 8 ? a >> 3 : b / 24, ln = a < NQ * 8 ? (a & 6) * 4 + 2 + (a & 1) : k / 6 * 8 + ((k % 6 + 4) & 7);
                    const u32 pos = P2 + (q >> 2), row = ((pos % 576) >> 5) * 64 + (pos / 576) * 32 + (pos & 31);
                    PREFETCH(" sc0 sc1", lbase + KA_JUNK, w2, ((q & 3) * 1152 + row) * (L_ * 2) + ln * 128); ++i2; }
                else __builtin_amdgcn_s_sleep(4);
            }
        }
        asm volatile("" : "+s"(lw));
        const char* gsrc = (const char*)(G8 + (size_t)d2 * 8) + (size_t)(kq * 144 + back * 64) * (D_ * 16) - 4096;
        auto issue_g = [&](int kt) {
            const char* gs = gsrc + kt * gstep;
            const u32 gd = lbase + 65536 + (kt & 3) * G2_GS + 4608 * wave;
            GLDS4(" sc1", gd, gs, goff[0], goff[1], goff[2], goff[3]);
            if (lane < 32) GLDS1(" sc1", gd + 4096, gs, goff[4]);
        };
        auto gather = [&]() {
            GLDS4(" sc1", lw + KB_RES, ws + WS_Y - 4096 + (tile >> 2) * 1024, ro[0], ro[1], ro[2], ro[3]);
            GLDS3("", lw + KB_RX - 1024 * wave, (const char*)(x + (s0 + 16 * kq) * D_ + d2) - 4096, ro[4], ro[5], ro[6]);
        };
        auto check_g = [&](int kt, auto more) {
            const u32 a = lbase + 65536 + (kt & 3) * G2_GS + 4608 * wave + lane * 16, a4 = a - (lane & 32) * 16;
            u32x4 v0, v1, v2, v3, v4;
            DS_R128(v0, a, 0); DS_R128(v1, a, 1024); DS_R128(v2, a, 2048); DS_R128(v3, a, 3072); DS_R128(v4, a4, 4096);
            more();
            for (;;) {
                asm volatile("s_waitcnt lgkmcnt(0)" : "+v"(v0), "+v"(v1), "+v"(v2), "+v"(v3), "+v"(v4) :: "memory");
                if (!__any(max5(max5(max5(max5(max5(0, v0), v1), v2), v3), v4) == ~0u) || late()) break;
                issue_g(kt); WAIT_V(0);
                DS_R128(v0, a, 0); DS_R128(v1, a, 1024); DS_R128(v2, a, 2048); DS_R128(v3, a, 3072); DS_R128(v4, a4, 4096);
            }
        };
        int pass = 0;
#pragma clang loop unroll(disable)
        for (;; pass = 1) {
        asm volatile("" : "+s"(pass), "+s"(gsrc), "+s"(w2src));
        if (!g1 && !pass && wave8 == 4) {
            while (__builtin_amdgcn_readfirstlane(poll_now((u32)(WS_EP - WS_CTL) + (HINT_G2 + xg * 32) * 4) ^ cnt[19]) && !late()) __builtin_amdgcn_s_sleep(2);
        }
        asm volatile("" ::: "memory");
        if (g1) lds_wait<1>(cnt + 21, 4u);
        else if (!pass) BAR();
        __builtin_amdgcn_s_sleep(G_DELAY);
        issue_g(0); issue_g(1);
        WAIT_V(5);
        check_g(0, [] {}); BAR();
        issue_g(2);
        WAIT_V(5);
        check_g(1, [&] { issue_g(3); }); BAR();
        if (g1 && !pass && wave8 == 5 && lane < 8)
            asm volatile("global_atomic_add %0, %1, %2" :: "v"((CTL_E4 + ((((fg / 9) * 4 + (2 * mt1 + (lane >> 2)) / 3) * 4 + (lane & 3)) * 4 + (fg & 3)) * 32) * 4), "v"(2u), "s"(ctl) : "memory");
        const char* wp = w2src + 4 * wstep; const char* gp = gsrc + 4 * gstep;
        const u32 lg = lbase + 65536 + 4608 * wave;
#pragma unroll
        for (int kt = 2; kt < 16; ++kt) {
            if (kt == NVG - 4 && wave8 == 4) poll_start(e4o);
            GLDS4("", lw + ((kt + 2) & 3) * 16384, wp, woff2[0], woff2[1], woff2[2], woff2[3]);
            GLDS4(" sc1", lg + ((kt + 2) & 3) * G2_GS, gp, goff[0], goff[1], goff[2], goff[3]);
            asm volatile("s_mov_b32 m0, %0\n\ts_mov_b32 exec_hi, 0\n\tglobal_load_lds_dwordx4 %2, %1 sc1\n\ts_mov_b32 exec_hi, -1" :: "s"(lg + ((kt + 2) & 3) * G2_GS + 4096), "s"(gp), "v"(goff[4]) : "memory");
            wp += kt < 6 ? wstep : kt == 6 ? (long)(back ? 32 : 32 - 1024) * (L_ * 2) : 128 * (L_ * 2);
            gp += kt < 6 ? gstep : kt == 6 ? (long)(back ? 72 : 8) * (D_ * 16) : 8 * (D_ * 16);
            asm volatile("" : "+s"(wp), "+s"(gp));
            if (kt == 2) WAIT_V(14); else WAIT_V(18);
            if (kt == NVG - 3 && wave8 == 4) e4_wait((int)poll_get(), 54);
            BAR();
        }
        code_again(32);
        WAIT_V(10);
        BAR();
        gather();
        WAIT_V(8);
        BAR();
        u32 redo;
        do { __builtin_amdgcn_s_sleep(1); redo = __builtin_amdgcn_readfirstlane(cnt[23]); } while ((redo & 0xff) < 4u);
        if (__builtin_expect(pass || redo < 0x100u, 1)) break;
        WAIT_V(0);
        BAR();
#pragma unroll
        for (int kt = 0; kt < 4; ++kt) issue_w2(w2src + kt * wstep, kt);
        }
    } else if (g1) {
        const int wf = wave >> 1, wd = wave & 1;
        u32 wa[4][2];
#pragma unroll
        for (int t = 0; t < 4; ++t)
#pragma unroll
            for (int tau = 0; tau < 2; ++tau) {
                int row = 8 * g + 4 * tau + q;
                int swz = (q << 2) | ((2 * g + tau) & 3);
                int ch = 2 * (4 * wf + t) + (p >> 1);
                wa[t][tau] = lbase + row * 256 + ((ch ^ swz) << 4) + 8 * (p & 1);
                PIN(wa[t][tau]);
            }
        u32 ha = lbase + 65536 + g * 1536 + (48 * wd + i16) * 16;
        u32 e3a = (CTL_E3 + (lane * 8 + blk / 36) * 32) * 4, one = 1;
        int go = (((fg / 9) * 144 + (fg % 9) * 8 + 4 * wf + 2 * (g & 1) + (g >> 1)) * D_ + d1 + 48 * wd + i16) * 16;
        asm volatile("" : "+v"(ha), "+v"(e3a), "+v"(one), "+v"(go));
        if (!early) __builtin_amdgcn_s_sleep(Y_DELAY);
        Y_LOAD("s_cmp_lt_u32 %9, 64\n\ts_cbranch_scc1 1f\n\t", "\n1:", blk);
        f32x4 acc[4][3];
        G1Set X, Y;
        G1Stage P0, P1;
        auto init = [&]() {
            u32x2 bw[4];
#pragma unroll
            for (int t = 0; t < 4; ++t) bw[t] = *(const u32x2*)(smem + KB_BIAS + (64 * wf + 16 * t + 4 * g) * 2);
#pragma unroll
            for (int t = 0; t < 4; ++t) {
#pragma unroll
                for (int m = 0; m < 3; ++m) { acc[t][m] = f32x4{bf_lo(bw[t][0]), bf_hi(bw[t][0]), bf_lo(bw[t][1]), bf_hi(bw[t][1])}; PIN(acc[t][m]); }
            }
        };
        init();
        if (!early) ln2(IC<3>{}, 2 * wave, wave8, [] {});
        WAIT_V(63); __builtin_amdgcn_sched_barrier(0);
        int pass = 0;
#pragma clang loop unroll(disable)
        for (;; pass = 1) {
        asm volatile("" : "+s"(pass));
        if (__builtin_expect(pass, 0)) { asm volatile("s_nop 7\n\ts_nop 7" ::: "memory"); init(); }
        BAR_L0();
        g1_half<0, 0, false>(acc, Y, X, wa, ha); WAIT_L0();
        g1_half<0, 1>(acc, X, Y, wa, ha);
        BAR_L0();
        g1_half<1, 0>(acc, Y, X, wa, ha); WAIT_L0();
        g1_half<1, 1>(acc, X, Y, wa, ha);
        WAIT_V(0);
        BAR_L0();
        for (int kt = 2; kt < 30; kt += 4) {
            if ((pass | wave8 | (kt ^ 2)) == 0)
                asm volatile("s_mov_b64 exec, %3\n\tglobal_atomic_add %0, %1, %2\n\ts_mov_b64 exec, -1" :: "v"(e3a), "v"(one), "s"(ctl), "i"((1ll << E3R) - 1) : "memory");
            g1_half<2, 0>(acc, Y, X, wa, ha); WAIT_L0();
            g1_half<2, 1>(acc, X, Y, wa, ha);
            BAR_L0();
            g1_half<3, 0>(acc, Y, X, wa, ha); WAIT_L0();
            g1_half<3, 1>(acc, X, Y, wa, ha);
            BAR_L0();
            g1_half<0, 0>(acc, Y, X, wa, ha); WAIT_L0();
            g1_half<0, 1>(acc, X, Y, wa, ha);
            BAR_L0();
            g1_half<1, 0>(acc, Y, X, wa, ha); WAIT_L0();
            g1_half<1, 1>(acc, X, Y, wa, ha);
            BAR_L0();
        }
#pragma unroll
        for (int i = 0; i < 12; ++i) { MFMA_BF(acc[i / 3][i % 3], Y.W[i / 3], Y.H[i % 3]); g1_take<2>(P0, i, wa, ha); }
        {
            const float sum = add3v(acc[0][0][0], acc[0][1][0], acc[0][2][0]);
            asm volatile("s_mov_b64 exec, %2\n\tds_write_b32 %0, %1\n\ts_mov_b64 exec, -1" :: "v"(lbase + KA_MISC + 256 + 22 * 4), "v"(one), "s"(__ballot(sum != sum)) : "memory");
        }
        BAR_L0();
        g1_part<0>(acc, P0, [&](int i) { g1_take<3>(P1, i, wa, ha); });
        BAR_L0();
        u32 redo;
        asm volatile("ds_read_b32 %0, %1" : "=v"(redo) : "v"(lbase + KA_MISC + 256 + 22 * 4) : "memory");
        g1_part<0>(acc, P1, [](int) {});
        asm volatile("s_waitcnt lgkmcnt(0)" : "+v"(redo));
        if (__builtin_expect(__builtin_amdgcn_readfirstlane(redo) <= (u32)pass, 1)) break;
        }
        FENCE6(acc[0][0], acc[0][1], acc[0][2], acc[1][0], acc[1][1], acc[1][2]);
        const __amdgpu_buffer_rsrc_t rs_g = __builtin_amdgcn_make_buffer_rsrc(G8, 0, D_ * F_ * 2, 0x00020000);
#pragma unroll
        for (int t = 0; t < 4; t += 2) {
#pragma unroll
            for (int m = 0; m < 3; ++m) {
                u32 o[2][2];
#pragma unroll
                for (int k = 0; k < 2; ++k) {
                    const f32x4 v = acc[t + k][m];
                    const f32x2 lo = gelu2(f32x2{v[0], v[1]}), hi = gelu2(f32x2{v[2], v[3]});
                    o[k][0] = pk_bf16(lo[0], lo[1]);
                    o[k][1] = pk_bf16(hi[0], hi[1]);
                }
                auto r0 = __builtin_amdgcn_permlane16_swap(o[0][0], o[1][0], false, false);
                auto r1 = __builtin_amdgcn_permlane16_swap(o[0][1], o[1][1], false, false);
                u32x4 gv = u32x4{r0[0], r1[0], r0[1], r1[1]};
                __builtin_amdgcn_raw_buffer_store_b128(t == 0 ? clean4(gv) : gv, rs_g, go + (36 * t * D_ + 16 * m) * 16, 0, SC1);
            }
            if (t == 0) {
                lds_arrive(cnt + 21, lane);
                if ((r5 | wave8) == 0) asm volatile("s_mov_b64 exec, 1\n\tglobal_store_dword %0, %1, %2 sc1\n\ts_mov_b64 exec, -1" :: "v"((u32)(WS_EP - WS_CTL) + (HINT_G2 + xg * 32) * 4), "v"(ytag), "s"(ctl) : "memory");
                asm volatile("s_nop 1" ::: "memory");
                g1_part<2>(acc, P0, [](int) {}); g1_part<2>(acc, P1, [](int) {});
                FENCE6(acc[2][0], acc[2][1], acc[2][2], acc[3][0], acc[3][1], acc[3][2]);
            }
        }
    }
    if (wave8 < 4 && !g1) BAR();
    const __amdgpu_buffer_rsrc_t rs_s = __builtin_amdgcn_make_buffer_rsrc(slabs + (size_t)tile * (16 * SLAB), 0, 16 * SLAB, 0x00020000);
    u32 po[3], oo[3];
#pragma unroll
    for (int e = 0; e < 3; ++e) {
        const int m = e < 2 ? (wave8 & 6) + e : 8, r = i16 + 16 * (wave8 & 1);
        oo[e] = ((s0 + 16 * kq + i16 + 64 * (wave8 & 1)) * D_ + d2 + 16 * m + (e == 0 ? 16 * (g & 1) + 8 * (g >> 1) : 4 * g)) * 2;
        po[e] = (kq * 4 + ((kq + 1 + e) & 3)) * SLAB + wave8 * 1024 + lane * 16;
    }
    asm volatile("" : "+v"(po[0]), "+v"(po[1]), "+v"(po[2]));
    u32x4 part[3]; u32x2 part8[3];
    const int p8 = wave8 < 2 ? 8192 - wave8 * (1024 - 8) : 0x40000000;
    auto fetch = [&]() {
#pragma unroll
        for (int k = 0; k < 3; ++k) part[k] = __builtin_amdgcn_raw_buffer_load_b128(rs_s, po[k], 0, SC1);
#pragma unroll
        for (int k = 0; k < 3; ++k) part8[k] = __builtin_amdgcn_raw_buffer_load_b64(rs_s, po[k], p8, SC1);
    };
    if (wave8 < 4) {
        u32 wa[2][2];
#pragma unroll
        for (int t = 0; t < 2; ++t)
#pragma unroll
            for (int tau = 0; tau < 2; ++tau) {
                int row = 8 * g + 4 * tau + q;
                int swz = (q << 2) | ((2 * g + tau) & 3);
                int ch = 2 * (wave + 4 * t) + (p >> 1);
                wa[t][tau] = lbase + row * 256 + ((ch ^ swz) << 4) + 8 * (p & 1);
            }
        const u32 ga0 = lbase + 65536 + g * 2304 + i16 * 16, ga2 = ga0 + 2 * G2_GS;
        f32x4 acc[9][2];
        G2Set X, Y;
        int so[3];
#pragma unroll
        for (int k = 0; k < 3; ++k) so[k] = (wave == kq ? 0x40000000 : (wave * 4 + kq) * SLAB) + lane * 16 + k * 4096;
        u32 fo = (CTL_XF + tile * 128 + wave * 32 + kq) * 4, one = 1;
        asm volatile("" : "+v"(so[0]), "+v"(so[1]), "+v"(so[2]), "+v"(fo), "+v"(one));

        int pass = 0;
#pragma clang loop unroll(disable)
        for (;; pass = 1) {
        asm volatile("" : "+s"(pass));
        BAR_L0();
        g2_half<0, 0, true, 1>(acc, Y, X, wa, ga0, ga2); WAIT_L0();
        g2_half<0, 1, true, 2>(acc, X, Y, wa, ga0, ga2);
        WAIT_V(0);
        for (int kt = 1; kt < 17; kt += 4) {
            BAR_L0();
            g2_half<1, 0, true>(acc, Y, X, wa, ga0, ga2); WAIT_L0();
            g2_half<1, 1, true>(acc, X, Y, wa, ga0, ga2);
            BAR_L0();
            g2_half<2, 0, true>(acc, Y, X, wa, ga0, ga2); WAIT_L0();
            g2_half<2, 1, true>(acc, X, Y, wa, ga0, ga2);
            BAR_L0();
            g2_half<3, 0, true>(acc, Y, X, wa, ga0, ga2); WAIT_L0();
            g2_half<3, 1, true>(acc, X, Y, wa, ga0, ga2);
            BAR_L0();
            g2_half<0, 0, true>(acc, Y, X, wa, ga0, ga2); WAIT_L0();
            g2_half<0, 1, true>(acc, X, Y, wa, ga0, ga2);
        }
        f32x2 chk = {0, 0};
        PIN(chk);
        BAR_L0();
        g2_half<1, 0, true, 3>(acc, Y, X, wa, ga0, ga2, &chk); WAIT_L0();
        {
            const float s1 = chk[0] + chk[1];
            const u32 inc = __any(s1 != s1) ? 0x101u : 1u;
            if (lane == 0) asm volatile("ds_add_u32 %0, %1" :: "v"(lbase + KA_MISC + 256 + 23 * 4), "v"(inc) : "memory");
        }
        g2_half<1, 1, true>(acc, X, Y, wa, ga0, ga2);
        WAIT_L0();
        u32 redo;
        asm volatile("ds_read_b32 %0, %1" : "=v"(redo) : "v"(lbase + KA_MISC + 256 + 23 * 4) : "memory");
        {
            u32 pc[9][4];
#pragma unroll
            for (int i = 0; i < 18 + SLAG; ++i) {
                if (i < 18) MFMA_BF(acc[i / 2][i % 2], Y.A[i / 2], Y.W[i % 2]);
                if (i == SLAG) asm volatile("s_nop %0" :: "i"(7 - SLAG));
                if (i == 18 + SLAG - 2) asm volatile("s_nop 1");
                if (i >= SLAG) {
                    const int j = i - SLAG, m = j / 2, t = j % 2, k = m < 8 ? (m & 6) + t : 8, h = m < 8 ? m & 1 : t;
                    asm volatile("v_cvt_pk_bf16_f32 %0, %2, %3\n\tv_cvt_pk_bf16_f32 %1, %4, %5" : "=&v"(pc[k][2 * h]), "=v"(pc[k][2 * h + 1])
                                 : "v"(acc[m][t][0]), "v"(acc[m][t][1]), "v"(acc[m][t][2]), "v"(acc[m][t][3]));
                    if (h) __builtin_amdgcn_raw_buffer_store_b128(u32x4{pc[k][0], pc[k][1], pc[k][2], pc[k][3]}, rs_s, so[k >> 2] + (k & 3) * 1024, 0, SC1);
                }
            }
        }
        asm volatile("s_waitcnt lgkmcnt(0)" : "+v"(redo));
        redo = __builtin_amdgcn_readfirstlane(redo);
        while (__builtin_expect((redo & 0xff) < 4u, 0)) redo = __builtin_amdgcn_readfirstlane(cnt[23]);
        if (__builtin_expect(pass || redo < 0x100u, 1)) break;
        BAR();
        asm volatile("s_nop 7\n\ts_nop 7");
        }
#pragma unroll
        for (int m = 0; m < 9; ++m)
#pragma unroll
            for (int t = 0; t < 2; ++t) PIN(acc[m][t]);
        if (wave == kq) {
            f32x4* own = (f32x4*)(smem + 32768);
#pragma unroll
            for (int i = 0; i < 18; ++i) own[i * 64 + lane] = acc[i >> 1][i & 1];
        } else {
            asm volatile("s_mov_b64 exec, 1\n\ts_waitcnt vmcnt(0)\n\tglobal_store_dword %0, %1, %2 sc1\n\ts_mov_b64 exec, -1" :: "v"(fo), "v"(one), "s"(ctl) : "memory");
        }
    } else {
        WAIT_V(0);
    }
    if (wave8 == 4) {
        const u32 fo = (CTL_XF + tile * 128 + kq * 32 + (lane & 3)) * 4;
        u32 f0, f1, f2;
#define LOOK(f) asm volatile("global_load_dword %0, %1, %2 sc1" : "=v"(f) : "v"(fo), "s"(ctl) : "memory")
#define SEEN(f) asm volatile("s_waitcnt vmcnt(2)" : "+v"(f) :: "memory"); if (__all(f == 1u || (lane & 3) == kq)) break
        LOOK(f0); __builtin_amdgcn_s_sleep(XF_GAP); LOOK(f1); __builtin_amdgcn_s_sleep(XF_GAP);
        for (;;) {
            LOOK(f2); SEEN(f0); __builtin_amdgcn_s_sleep(XF_GAP);
            LOOK(f0); SEEN(f1); __builtin_amdgcn_s_sleep(XF_GAP);
            LOOK(f1); SEEN(f2); __builtin_amdgcn_s_sleep(XF_GAP);
            if (late()) break;
        }
        BAR();
        fetch();
        asm volatile("s_waitcnt vmcnt(6)" : "+v"(f0), "+v"(f1), "+v"(f2) :: "memory");
    } else {
        BAR_L0();
        fetch();
    }
    asm volatile("" ::: "memory");
    const float bb = bf2f(*(const u16*)(smem + KB_BIAS + 256 + (16 * kq + i16 + 64 * (wave8 & 1)) * 2));
    residual();
    const __amdgpu_buffer_rsrc_t rs_o = __builtin_amdgcn_make_buffer_rsrc(out, 0, L_ * D_ * 2, 0x00020000);
    u32x2 ov[3];
    f32x4 own[3];
#pragma unroll
    for (int e = 0; e < 3; ++e) own[e] = ((const f32x4*)(smem + 32768))[((e < 2 ? (wave8 & 6) + e : 8) * 2 + (wave8 & 1)) * 64 + lane];
#pragma unroll
    for (int e = 0; e < 3; ++e) own[e] += bb;
    asm volatile("" : "+v"(own[0]), "+v"(own[1]), "+v"(own[2]), "+v"(hv[0]), "+v"(hv[1]), "+v"(hv[2]), "+v"(oo[0]), "+v"(oo[2]));
#pragma unroll
    for (int e = 0; e < 3; ++e) {
        if (e == 2 && wave8 >= 2) break;
        f32x4 v = own[e];
#pragma unroll
        for (int k = 0; k < 3; ++k) {
            const u32 a = e < 2 ? part[k][2 * e] : part8[k][0], b = e < 2 ? part[k][2 * e + 1] : part8[k][1];
            v += f32x4{bf_lo(a), bf_hi(a), bf_lo(b), bf_hi(b)};
        }
        const u32 y0 = pk_bf16(v[0], v[1]), y1 = pk_bf16(v[2], v[3]);
        ov[e] = u32x2{pk_bf16(bf_lo(hv[e][0]) + bf_lo(y0), bf_hi(hv[e][0]) + bf_hi(y0)), pk_bf16(bf_lo(hv[e][1]) + bf_lo(y1), bf_hi(hv[e][1]) + bf_hi(y1))};
        if (e == 1) {
            auto r0 = __builtin_amdgcn_permlane16_swap(ov[0][0], ov[1][0], false, false);
            auto r1 = __builtin_amdgcn_permlane16_swap(ov[0][1], ov[1][1], false, false);
            __builtin_amdgcn_raw_buffer_store_b128(u32x4{r0[0], r1[0], r0[1], r1[1]}, rs_o, oo[0], 0, SC1);
        }
        if (e == 2) __builtin_amdgcn_raw_buffer_store_b64(ov[2], rs_o, oo[2], 0, SC1);
    }
}
__global__ void k_end() { asm volatile(".Lcode_end:"); }

extern "C" void kernel_launch(void* const* d_in, const int* in_sizes, int n_in,
                              void* d_out, int out_size, void* d_ws, size_t ws_size,
                              hipStream_t stream) {
    const u16* x = (const u16*)d_in[0];
    const u16* logA = (const u16*)d_in[1];
    const u16* Aim = (const u16*)d_in[2];
    const u16* Cre = (const u16*)d_in[3];
    const u16* Cim = (const u16*)d_in[4];
    const u16* logdt = (const u16*)d_in[5];
    const u16* Dskip = (const u16*)d_in[6];
    const u16* w1 = (const u16*)d_in[8];
    const u16* b1 = (const u16*)d_in[9];
    const u16* w2 = (const u16*)d_in[10];
    const u16* b2 = (const u16*)d_in[11];
    char* ws = (char*)d_ws;
    (void)hipFuncSetAttribute((const void*)k_all, hipFuncAttributeMaxDynamicSharedMemorySize, ALL_LDS);
    k_all<<<256, 512, ALL_LDS, stream>>>(x, ws, logA, Aim, Cre, Cim, logdt, Dskip, w1, b1, w2, b2, (u16*)d_out);
}
```
